```python
import torch

def _softmax(x, axis=-1):
    return torch.softmax(x, dim=axis)
def _gelu(x):
    return torch.nn.functional.gelu(x, approximate="tanh")
def _silu(x):
    return torch.nn.functional.silu(x)
def _sigmoid(x):
    return torch.sigmoid(x)
def _layernorm(x, axis=-1, eps=1e-5):
    m = x.mean(dim=axis, keepdim=True)
    v = x.var(dim=axis, keepdim=True, unbiased=False)
    return (x - m) / torch.sqrt(v + eps)
def _scatter_add(tgt, idx, src, axis=0):
    return tgt.clone().index_add_(axis, idx, src)
def _segment_sum(src, seg, n):
    out = torch.zeros((n,) + src.shape[1:], dtype=src.dtype)
    return out.index_add_(0, seg, src)
def _pad(x, pw):
    flat = tuple(v for pair in reversed(pw) for v in pair)
    return torch.nn.functional.pad(x, flat)

kernel_name = 'parallel_sum__sliding_block_cached__dense_activation_silu__decode'

def setup_inputs():

    _w = (lambda a: (lambda t: t.to(torch.bfloat16) if t.is_floating_point() else t)(torch.as_tensor(a)))
    def _A_setup():
        B, Sq, D = 1, 1, 1024
        Hq, Hkv, Sk, Dh = 32, 4, 65536, 128
        assert Hq % Hkv == 0, (Hq, Hkv)
        return {
            "x": _w((lambda *s: torch.randn(*s, dtype=torch.bfloat16) * 0.05)(B, Sq, D)),
            "k_cache": _w((lambda *s: torch.randn(*s, dtype=torch.bfloat16) * 3.2)(B, Hkv, Sk, Dh)),
            "v_cache": _w((lambda *s: torch.randn(*s, dtype=torch.bfloat16) * 0.8)(B, Hkv, Sk, Dh)),
            "wq": _w((lambda *s: torch.randn(*s, dtype=torch.bfloat16) * 3.2)(D, Hq * Dh) * (D ** -0.5)),
            "wo": _w((lambda *s: torch.randn(*s, dtype=torch.bfloat16) * 0.8)(Hq * Dh, D) * ((Hq * Dh) ** -0.5)),
        }
    _A_in = _A_setup()

    def _B_setup():
        B, S, D, F = 1, 1, 1024, 3584
        return {
            "x": _w((lambda *s: torch.randn(*s, dtype=torch.bfloat16) * 0.05)(B, S, D)),
            "w1": _w((lambda *s: torch.randn(*s, dtype=torch.bfloat16) * 3.2)(D, F) * (D ** -0.5)),
            "w2": _w((lambda *s: torch.randn(*s, dtype=torch.bfloat16) * 0.2)(F, D) * (F ** -0.5)),
        }
    _B_in = _B_setup()

    return {
        "A_x": _A_in["x"],
        "A_k_cache": _A_in["k_cache"],
        "A_v_cache": _A_in["v_cache"],
        "A_wq": _A_in["wq"],
        "A_wo": _A_in["wo"],
        "B_x": _B_in["x"],
        "B_w1": _B_in["w1"],
        "B_w2": _B_in["w2"],
    }

def reference(A_x, A_k_cache, A_v_cache, A_wq, A_wo, B_x, B_w1, B_w2):
    def _A_ref(x, k_cache, v_cache, wq, wo):
        B, Sq, D = x.shape
        Hq, Dh = 32, 128
        Hkv, Sk = k_cache.shape[1], k_cache.shape[2]
        W = min(4096, max(256, Sk // 4))
        rep = Hq // Hkv
        q = (lambda x, *d: x.permute(*d))((x @ wq).reshape(B, Sq, Hq, Dh), 0, 2, 1, 3)
        k = (lambda x, n, axis: torch.repeat_interleave(x, n, dim=axis))(k_cache[:, :, Sk - W:, :], rep, axis=1)
        v = (lambda x, n, axis: torch.repeat_interleave(x, n, dim=axis))(v_cache[:, :, Sk - W:, :], rep, axis=1)
        scores = torch.einsum("bhqd,bhkd->bhqk", q, k) * (Dh ** -0.5)
        p = _softmax(scores, axis=-1)
        o = torch.einsum("bhqk,bhkd->bhqd", p, v)
        return (lambda x, *d: x.permute(*d))(o, 0, 2, 1, 3).reshape(B, Sq, Hq * Dh) @ wo

    def _B_ref(x, w1, w2):
        h = torch.einsum("bsd,df->bsf", x, w1)
        h = _silu(h)
        out = torch.einsum("bsf,fd->bsd", h, w2)
        return out

    x = A_x
    xn = _layernorm(x, axis=-1)
    return x + _A_ref(xn, A_k_cache, A_v_cache, A_wq, A_wo) + _B_ref(xn, B_w1, B_w2)
```

```cpp
#include <hip/hip_runtime.h>
#include <hip/hip_bf16.h>
#define MIMIC 0
typedef unsigned short u16;
__device__ __forceinline__ float bf2f(u16 b){ return __uint_as_float(((unsigned)b)<<16); }
__device__ __forceinline__ u16 f2bf(float f){ unsigned u=__float_as_uint(f); u += 0x7fffu + ((u>>16)&1u); return (u16)(u>>16);}
__device__ __forceinline__ float rb(float f){ return MIMIC ? bf2f(f2bf(f)) : f; }
constexpr int D=1024, HQ=32, HKV=4, SK=65536, DH=128, W=4096, F=3584;

__device__ void ln(const u16* x, float* xs, float* red){
  int t=threadIdx.x;
  float v[4]; float s=0;
  for(int i=0;i<4;i++){ v[i]=bf2f(x[t*4+i]); s+=v[i]; }
  red[t]=s; __syncthreads();
  for(int o=128;o>0;o>>=1){ if(t<o) red[t]+=red[t+o]; __syncthreads(); }
  float mean=red[0]/D; __syncthreads();
  float q=0; for(int i=0;i<4;i++){ float d=v[i]-mean; q+=d*d; }
  red[t]=q; __syncthreads();
  for(int o=128;o>0;o>>=1){ if(t<o) red[t]+=red[t+o]; __syncthreads(); }
  float var=red[0]/D; __syncthreads();
  float mb=rb(mean), vb=rb(var);
  float den=rb(sqrtf(rb(vb+1e-5f)));
  for(int i=0;i<4;i++){ xs[t*4+i]=rb(rb(v[i]-mb)/den); }
  __syncthreads();
}
__global__ void k1(const u16* x,const u16* wq,const u16* w1,float* q,float* h){
  __shared__ float xs[1024]; __shared__ float red[256];
  ln(x,xs,red);
  int t=threadIdx.x; int b=blockIdx.x;
  const u16* w; int ld; int col;
  if(b<128){ w=wq; ld=4096; col=b*32+(t&31);} else { w=w1; ld=F; col=(b-128)*32+(t&31);}
  int rg=t>>5; float a=0;
  for(int r=rg*128;r<rg*128+128;r++) a+=xs[r]*bf2f(w[(size_t)r*ld+col]);
  red[t]=a; __syncthreads();
  if(t<32){ float s=0; for(int g=0;g<8;g++) s+=red[g*32+t];
    if(b<128) q[col]=rb(s); else { float z=rb(s); h[col]=rb(z/(1.f+expf(-z))); } }
}
__global__ void k2(const float* q,const u16* kc,const u16* vc,float* o){
  __shared__ float sc[W]; __shared__ float qs[DH]; __shared__ float red[256];
  int h=blockIdx.x, t=threadIdx.x, g=h/8;
  if(t<DH) qs[t]=q[h*DH+t]; __syncthreads();
  const u16* K=kc+((size_t)g*SK+(SK-W))*DH; const u16* V=vc+((size_t)g*SK+(SK-W))*DH;
  float m=-INFINITY;
  for(int k=t;k<W;k+=256){ float a=0; for(int d=0;d<DH;d++) a+=qs[d]*bf2f(K[(size_t)k*DH+d]); a=rb(rb(a)*0.08838834764831845f); sc[k]=a; m=fmaxf(m,a);}
  red[t]=m; __syncthreads();
  for(int s=128;s>0;s>>=1){ if(t<s) red[t]=fmaxf(red[t],red[t+s]); __syncthreads(); }
  m=red[0]; __syncthreads();
  float l=0; for(int k=t;k<W;k+=256){ float e=expf(sc[k]-m); sc[k]=e; l+=e; }
  red[t]=l; __syncthreads();
  for(int s=128;s>0;s>>=1){ if(t<s) red[t]+=red[t+s]; __syncthreads(); }
  l=red[0]; __syncthreads();
  int d=t&127, half=t>>7; float a=0;
  for(int k=half*2048;k<half*2048+2048;k++) a+=rb(sc[k]/l)*bf2f(V[(size_t)k*DH+d]);
  red[t]=a; __syncthreads();
  if(t<128) o[h*DH+t]=rb(red[t]+red[t+128]);
}
__global__ void k3(const u16* x,const float* o,const float* h,const u16* wo,const u16* w2,u16* out){
  __shared__ float red[256], red2[256];
  int t=threadIdx.x; int col=blockIdx.x*32+(t&31); int rg=t>>5;
  float a=0,b=0;
  for(int r=rg*512;r<rg*512+512;r++) a+=o[r]*bf2f(wo[(size_t)r*D+col]);
  for(int r=rg*448;r<rg*448+448;r++) b+=h[r]*bf2f(w2[(size_t)r*D+col]);
  red[t]=a; red2[t]=b; __syncthreads();
  if(t<32){ float s=0,s2=0; for(int g=0;g<8;g++){ s+=red[g*32+t]; s2+=red2[g*32+t]; }
    out[col]=f2bf(rb(bf2f(x[col])+rb(s))+rb(s2)); }
}
extern "C" void kernel_launch(void* const* d_in, const int* in_sizes, int n_in, void* d_out, int out_size, void* d_ws, size_t ws_size, hipStream_t stream){
  const u16* x=(const u16*)d_in[0]; const u16* kc=(const u16*)d_in[1]; const u16* vc=(const u16*)d_in[2];
  const u16* wq=(const u16*)d_in[3]; const u16* wo=(const u16*)d_in[4]; const u16* w1=(const u16*)d_in[6]; const u16* w2=(const u16*)d_in[7];
  float* q=(float*)d_ws; float* h=q+4096; float* o=h+4096;
  k1<<<240,256,0,stream>>>(x,wq,w1,q,h);
  k2<<<32,256,0,stream>>>(q,kc,vc,o);
  k3<<<32,256,0,stream>>>(x,o,h,wo,w2,(u16*)d_out);
}
```

```cpp
#include <hip/hip_runtime.h>
#include <hip/hip_bf16.h>

#define LAS __attribute__((address_space(3)))
typedef unsigned v4u __attribute__((ext_vector_type(4)));
typedef unsigned v2u __attribute__((ext_vector_type(2)));
typedef float f4 __attribute__((ext_vector_type(4)));
typedef float f2 __attribute__((ext_vector_type(2)));
typedef __bf16 bf2 __attribute__((ext_vector_type(2)));
typedef __bf16 bf8 __attribute__((ext_vector_type(8)));
typedef short s4 __attribute__((ext_vector_type(4)));
typedef __amdgpu_buffer_rsrc_t rsrc_t;

constexpr int AUX_NT = 2, AUX_SC1 = 16;
constexpr unsigned WS_Q = 4096, WS_H = WS_Q + 65536, WS_O = WS_H + 65536, WS_R = WS_O + 1048576, WS_BYTES = WS_R + 262144;
constexpr int KROW = 272;
constexpr int L_CNT = 0, L_QM = 64, L_HM = L_QM + 2048, L_K = L_HM + 2048, L_V = L_K + 64 * KROW, L_Q = L_V + 64 * KROW,
              L_S = L_Q + 8 * KROW, L_X2 = L_S + 8 * 144, L_X3 = L_X2 + 128, L_END = L_X3 + 128;
constexpr int C_QM = 0, C_HM = 1, C_KV = 2, C_Q = 3, C_S = 4, C_X2 = 5, C_X3 = 6, C_HP = 7;
constexpr int CODE_LINES = 64;
#define PF_BLOCKS 0
constexpr int GSPIN = 1 << 14, LSPIN = 1 << 19;

#define FENCE() do { asm volatile("" ::: "memory"); __builtin_amdgcn_sched_barrier(0); } while (0)
#define PIN(x) asm volatile("" : "+v"(x))

#define STAMP(k)
#define STAMP_OUT()

__device__ __forceinline__ rsrc_t mkrs(const void* p, unsigned n) { return __builtin_amdgcn_make_buffer_rsrc((void*)p, 0, n, 0x00020000); }
template <int AUX> __device__ __forceinline__ v4u ld16(rsrc_t rs, unsigned v, unsigned s) { return __builtin_amdgcn_raw_buffer_load_b128(rs, v, s, AUX); }
__device__ __forceinline__ void st16(v4u d, rsrc_t rs, unsigned v) { __builtin_amdgcn_raw_buffer_store_b128(d, rs, v, 0, AUX_SC1); }
__device__ __forceinline__ float asf(unsigned u) { return __builtin_bit_cast(float, u); }
__device__ __forceinline__ unsigned asu(float f) { return __builtin_bit_cast(unsigned, f); }
__device__ __forceinline__ unsigned pk(float lo, float hi) { f2 v = {lo, hi}; return __builtin_bit_cast(unsigned, __builtin_convertvector(v, bf2)); }
__device__ __forceinline__ float lo16(unsigned u) { return asf(u << 16); }
__device__ __forceinline__ float hi16(unsigned u) { return asf(u & 0xffff0000u); }
__device__ __forceinline__ float rbf(float f) { return lo16(pk(f, f)); }
__device__ __forceinline__ f4 mfma(v4u a, v4u b, f4 c) { return __builtin_amdgcn_mfma_f32_16x16x32_bf16(__builtin_bit_cast(bf8, a), __builtin_bit_cast(bf8, b), c, 0, 0, 0); }
template <int CTRL> __device__ __forceinline__ unsigned dppu(unsigned x) { return (unsigned)__builtin_amdgcn_mov_dpp((int)x, CTRL, 0xf, 0xf, true); }
template <int CTRL> __device__ __forceinline__ float dpp(float x) { return asf(dppu<CTRL>(asu(x))); }
constexpr int XOR1 = 0xB1, XOR2 = 0x4E, ROR4 = 0x124, ROR8 = 0x128;
__device__ __forceinline__ float sum16(float x) { auto s = __builtin_amdgcn_permlane16_swap(asu(x), asu(x), false, false); return asf(s[0]) + asf(s[1]); }
__device__ __forceinline__ float sum32(float x) { auto s = __builtin_amdgcn_permlane32_swap(asu(x), asu(x), false, false); return asf(s[0]) + asf(s[1]); }
__device__ __forceinline__ float max16(float x) { auto s = __builtin_amdgcn_permlane16_swap(asu(x), asu(x), false, false); return fmaxf(asf(s[0]), asf(s[1])); }
__device__ __forceinline__ float max32(float x) { auto s = __builtin_amdgcn_permlane32_swap(asu(x), asu(x), false, false); return fmaxf(asf(s[0]), asf(s[1])); }
__device__ __forceinline__ float wave_sum(float x) { x += dpp<XOR1>(x); x += dpp<XOR2>(x); x += dpp<ROR4>(x); x += dpp<ROR8>(x); return sum32(sum16(x)); }

template <int OFF> __device__ __forceinline__ unsigned lds_add() {
  unsigned o, t;
  asm volatile("s_mov_b64 exec, 1\n\tds_add_rtn_u32 %1, %2, %3 offset:%4\n\ts_mov_b64 exec, -1\n\ts_waitcnt lgkmcnt(0)\n\tv_readfirstlane_b32 %0, %1"
               : "=s"(o), "=&v"(t) : "v"(0u), "v"(1u), "i"(OFF) : "memory");
  return o;
}
template <int OFF> __device__ __forceinline__ void lds_sig() {
  asm volatile("s_mov_b64 exec, 1\n\tds_add_u32 %0, %1 offset:%2\n\ts_mov_b64 exec, -1" :: "v"(0u), "v"(1u), "i"(OFF) : "memory");
}
template <int OFF, int WANT, int SLEEP> __device__ __forceinline__ void lds_wait() {
  unsigned n = LSPIN, t;
  asm volatile("1:\n\tds_read_b32 %1, %2 offset:%3\n\ts_sub_u32 %0, %0, 1\n\ts_waitcnt lgkmcnt(0)\n\tv_cmp_gt_u32 vcc, %4, %1\n\ts_cbranch_vccz 2f\n\t"
               "s_sleep %5\n\ts_cbranch_scc0 1b\n2:"
               : "+s"(n), "=&v"(t) : "v"(0u), "i"(OFF), "i"(WANT), "i"(SLEEP) : "memory", "vcc", "scc");
}
__device__ __forceinline__ unsigned load_tag(const unsigned* ws) {
  unsigned e;
  asm volatile("s_load_dword %0, %1, 0x0\n\ts_waitcnt lgkmcnt(0)" : "=s"(e) : "s"(ws) : "memory");
  return e + 0x01010101u;
}
__device__ __forceinline__ unsigned tag8(float v, unsigned tb) { return ((asu(v) + 0x80u) & 0xffffff00u) | tb; }

template <int area, int cnt> __device__ __forceinline__ bool merge_publish(LAS char* L, f4 d, int w, int l, rsrc_t rws, unsigned dst, unsigned T) {
  const int up = l >> 4, n = l & 15;
  if ((up >> 1) == (n >> 3)) *(LAS f4*)(L + area + w * 512 + (n >> 3) * 256 + (n & 7) * 32 + (up & 1) * 16) = d;
  const unsigned old = lds_add<4 * cnt>();
  if (old != 3u) return false;
  f4 a = *(LAS f4*)(L + area + up * 512 + n * 16), b = *(LAS f4*)(L + area + up * 512 + 256 + n * 16);
  a += b;
  v4u o;
#pragma unroll
  for (int k = 0; k < 4; k++) o[k] = tag8(sum32(sum16(a[k])), (T >> (8 * k)) & 0xffu);
  if (l < 16) st16(o, rws, dst + 16 * l);
  return true;
}

__global__ void __launch_bounds__(576)
fused_decode(const void* __restrict__ px, const void* __restrict__ pwq, unsigned* ws, const void* __restrict__ pk_, const void* __restrict__ pv,
             const void* __restrict__ pw1, const void* __restrict__ pw2, const void* __restrict__ pwo, void* __restrict__ pout) {
  __shared__ __attribute__((aligned(16))) char lds_[L_END];
  unsigned pfx;
  {
    unsigned long long pc, ex;
    asm volatile("s_getpc_b64 %1\n\ts_andn2_b64 %1, %1, 0xff\n\tv_lshlrev_b32 %0, 7, %3\n\tv_cmp_gt_u32 vcc, %4, %3\n\ts_and_saveexec_b64 %2, vcc\n\t"
                 "s_cbranch_execz 1f\n\tglobal_load_dword %0, %0, %1\n1:\n\ts_mov_b64 exec, %2"
                 : "=&v"(pfx), "=&s"(pc), "=&s"(ex) : "v"(threadIdx.x), "s"(blockIdx.x < PF_BLOCKS ? CODE_LINES : 0) : "vcc", "scc", "memory");
  }
  LAS char* L = (LAS char*)lds_;
  LAS unsigned* CNT = (LAS unsigned*)(L + L_CNT);
  const int tid = threadIdx.x, l = tid & 63, wave = __builtin_amdgcn_readfirstlane(tid >> 6);
  const int b = blockIdx.x, xcd = b & 7, loc = b >> 3, j = xcd * 32 + loc, cb = j >> 2, r = j & 3, g = j >> 6, s = j & 63;
  const int quad = loc >> 2, fb = xcd * 7 + quad, fb2 = ((xcd + 1) & 7) * 7 + quad;
  const int cbq = (cb + 16) & 63, cbo = (cb + 32) & 63;
  const bool hasF = quad < 7;
  STAMP(0);
  const rsrc_t rws = mkrs(ws, WS_BYTES);

  if (wave >= 8) {
    __builtin_amdgcn_s_setprio(2);
    const unsigned T = load_tag(ws);
    __builtin_amdgcn_s_barrier();
    FENCE();
    if (hasF) {
      lds_wait<4 * C_HP, 1, 4>();
      STAMP(1);
      v4u hp;
      int np = 0;
#pragma unroll 1
      for (int n = 0; n < GSPIN; n++) {
        FENCE();
        hp = ld16<AUX_SC1>(rws, WS_H + fb2 * 1024 + 16 * l, 0);
        FENCE();
        unsigned bad = 0;
#pragma unroll
        for (int k = 0; k < 4; k++) { hp[k] ^= (T >> (8 * k)) & 0xffu; bad |= hp[k]; }
        np++;
        if (!__any((bad & 0xffu) != 0u)) break;
      }
      STAMP(2);
      float hv[4];
#pragma unroll
      for (int k = 0; k < 4; k++) {
        const float z = rbf(sum32(sum16(asf(hp[k]))));
        hv[k] = z * __builtin_amdgcn_rcpf(1.f + __builtin_amdgcn_exp2f(z * -1.4426950408889634f));
      }
      if (l < 16) *(LAS v2u*)(L + L_X2 + 8 * l) = v2u{pk(hv[0], hv[1]), pk(hv[2], hv[3])};
      lds_sig<4 * C_X2>();
      STAMP(3);
    } else {
      const unsigned col = 256 * r + 64 * (xcd >> 1) + 8 * (l & 7) + 4 * (xcd & 1);
      const v2u xr = __builtin_amdgcn_raw_buffer_load_b64(mkrs(px, 2048), 2 * col, 0, 0);
      lds_wait<4 * C_X3, 4, 4>();
      STAMP(1);
      const unsigned ro = WS_R + (unsigned)(r * 8 + xcd) * 8192u + 16 * l;
      v4u rp[8];
      int np = 0;
#pragma unroll 1
      for (int n = 0; n < GSPIN; n++) {
        FENCE();
#pragma unroll
        for (int p = 0; p < 8; p++) rp[p] = ld16<AUX_SC1>(rws, ro, p * 1024);
        FENCE();
        unsigned bad = 0;
#pragma unroll
        for (int p = 0; p < 8; p++)
#pragma unroll
          for (int k = 0; k < 4; k++) { rp[p][k] ^= (T >> (8 * k)) & 0xffu; bad |= rp[p][k]; }
        np++;
        if (!__any((bad & 0xffu) != 0u)) break;
      }
      STAMP(2);
      float y[4];
#pragma unroll
      for (int k = 0; k < 4; k++) {
        float t = ((asf(rp[0][k]) + asf(rp[1][k])) + (asf(rp[2][k]) + asf(rp[3][k]))) + ((asf(rp[4][k]) + asf(rp[5][k])) + (asf(rp[6][k]) + asf(rp[7][k])));
        t += dpp<ROR8>(t);
        y[k] = sum32(sum16(t));
      }
      const v2u res = {pk(y[0] + lo16(xr[0]), y[1] + hi16(xr[0])), pk(y[2] + lo16(xr[1]), y[3] + hi16(xr[1]))};
      if (l < 8) __builtin_amdgcn_raw_buffer_store_b64(res, mkrs(pout, 2048), 2 * col, 0, 0);
      if (j == 28 && l == 0) __builtin_amdgcn_raw_buffer_store_b32(T, rws, 0, 0, AUX_SC1);
      STAMP(3);
    }
    STAMP_OUT();
  } else if (wave >= 4) {
    const int w = wave - 4, u = l >> 4, c = l & 15;
    __builtin_amdgcn_s_setprio(3);
    if (tid < 256 + 16) CNT[l] = 0u;
    const unsigned T = load_tag(ws);
    __builtin_amdgcn_s_barrier();
    FENCE();
    lds_wait<4 * C_KV, 4, 2>();
    STAMP(1);
    const unsigned qo = WS_Q + g * 16384 + w * 1024 + 16 * l;
    v4u qp[4];
#pragma unroll
    for (int p = 0; p < 4; p++) qp[p] = ld16<AUX_SC1>(rws, qo, p * 4096);
    FENCE();
    v4u Kf[4], Vf[2][2];
#pragma unroll
    for (int k = 0; k < 4; k++) Kf[k] = *(LAS v4u*)(L + L_K + (16 * w + c) * KROW + 64 * k + 16 * u);
#pragma unroll
    for (int t = 0; t < 2; t++)
#pragma unroll
      for (int h = 0; h < 2; h++) {
        LAS char* p = L + L_V + (32 * h + 8 * u + (c >> 2)) * KROW + 64 * w + 32 * t + 8 * (c & 3);
        const v2u e0 = __builtin_bit_cast(v2u, __builtin_amdgcn_ds_read_tr16_b64_v4i16((LAS s4*)p));
        const v2u e1 = __builtin_bit_cast(v2u, __builtin_amdgcn_ds_read_tr16_b64_v4i16((LAS s4*)(p + 4 * KROW)));
        Vf[t][h] = v4u{e0[0], e0[1], e1[0], e1[1]};
      }
    FENCE();
    int npoll = 0;
#pragma unroll 1
    for (int n = 0; n < GSPIN; n++) {
      unsigned bad = 0;
#pragma unroll
      for (int p = 0; p < 4; p++)
#pragma unroll
        for (int k = 0; k < 4; k++) { qp[p][k] ^= (T >> (8 * k)) & 0xffu; bad |= qp[p][k]; }
      npoll++;
      if (!__any((bad & 0xffu) != 0u)) break;
      FENCE();
#pragma unroll
      for (int p = 0; p < 4; p++) qp[p] = ld16<AUX_SC1>(rws, qo, p * 4096);
      FENCE();
    }
    STAMP(2);
    {
      float q4[4];
#pragma unroll
      for (int k = 0; k < 4; k++) q4[k] = (asf(qp[0][k]) + asf(qp[1][k])) + (asf(qp[2][k]) + asf(qp[3][k]));
      *(LAS v2u*)(L + L_Q + (2 * w + (l >> 5)) * KROW + 8 * (l & 31)) = v2u{pk(q4[0], q4[1]), pk(q4[2], q4[3])};
    }
    lds_sig<4 * C_Q>();
    lds_wait<4 * C_Q, 4, 0>();
    STAMP(3);
    f4 sc = {0.f, 0.f, 0.f, 0.f};
#pragma unroll
    for (int k = 0; k < 4; k++) sc = mfma(Kf[k], *(LAS v4u*)(L + L_Q + (c & 7) * KROW + 64 * k + 16 * u), sc);
    {
      const unsigned s0 = pk(sc[0], sc[1]), s1 = pk(sc[2], sc[3]);
      constexpr float SC = 0.08838834764831845f;
      if (c < 8) *(LAS v2u*)(L + L_S + c * 144 + 32 * w + 8 * u) = v2u{pk(lo16(s0) * SC, hi16(s0) * SC), pk(lo16(s1) * SC, hi16(s1) * SC)};
    }
    lds_sig<4 * C_S>();
    lds_wait<4 * C_S, 4, 0>();
    STAMP(4);
    constexpr float LOG2E = 1.4426950408889634f;
    {
      const v4u sv = *(LAS v4u*)(L + L_S + (c & 7) * 144 + 64 * (c >> 3) + 16 * u);
      float e[8];
#pragma unroll
      for (int k = 0; k < 4; k++) { e[2 * k] = lo16(sv[k]); e[2 * k + 1] = hi16(sv[k]); }
      float m = fmaxf(fmaxf(fmaxf(e[0], e[1]), fmaxf(e[2], e[3])), fmaxf(fmaxf(e[4], e[5]), fmaxf(e[6], e[7])));
      m = fmaxf(m, dpp<ROR8>(m));
      m = max32(max16(m));
      float ls = 0.f;
#pragma unroll
      for (int k = 0; k < 8; k++) { e[k] = __builtin_amdgcn_exp2f((e[k] - m) * LOG2E); ls += e[k]; }
      v4u P, Pr;
#pragma unroll
      for (int k = 0; k < 4; k++) { P[k] = pk(e[2 * k], e[2 * k + 1]); Pr[k] = dppu<ROR8>(P[k]); }
      const f4 z = {0.f, 0.f, 0.f, 0.f};
      f4 d0 = mfma(Vf[0][0], P, z), d1 = mfma(Vf[1][0], Pr, z);
      d0 = mfma(Vf[0][1], Pr, d0); d1 = mfma(Vf[1][1], P, d1);
      ls += dpp<ROR8>(ls);
      ls = sum32(sum16(ls));
      const f4 dd = (c < 8) ? d0 : d1;
      const v4u gr = {pk(dd[0], dd[1]), asu(m) | (T & 0xffffu), pk(dd[2], dd[3]), (pk(ls, ls) & 0xffff0000u) | (T >> 16)};
      const int src = 4 * (16 * (l & 3) + 8 * ((l >> 2) & 1) + (l >> 3));
      v4u o;
#pragma unroll
      for (int k = 0; k < 4; k++) o[k] = (unsigned)__builtin_amdgcn_ds_bpermute(src, (int)gr[k]);
      st16(o, rws, WS_O + (unsigned)(((16 * g + 2 * (l >> 3) + (w >> 1)) * 4 + 2 * (w & 1) + ((l >> 2) & 1)) * 64 + s) * 64u + 16 * (l & 3));
    }
    FENCE();
    STAMP(5);
    const unsigned oo = WS_O + (unsigned)(cbo * 4 + w) * 4096u + 16 * l;
    asm volatile("s_waitcnt vmcnt(0)" ::: "memory");
    int npoll2 = 0;
#pragma unroll 1
    for (int n = 0; n < GSPIN; n++) {
      FENCE();
#pragma unroll
      for (int p = 0; p < 4; p++) qp[p] = ld16<AUX_SC1>(rws, oo, p * 1024);
      FENCE();
      unsigned bad = 0;
#pragma unroll
      for (int p = 0; p < 4; p++) { qp[p][1] ^= T & 0xffffu; qp[p][3] ^= T >> 16; bad |= qp[p][1] | qp[p][3]; }
      npoll2++;
      if (!__any((bad & 0xffffu) != 0u)) break;
    }
    STAMP(6);
    {
      float M = fmaxf(fmaxf(asf(qp[0][1]), asf(qp[1][1])), fmaxf(asf(qp[2][1]), asf(qp[3][1])));
      M = fmaxf(M, dpp<ROR4>(M)); M = fmaxf(M, dpp<ROR8>(M));
      M = max32(max16(M));
      float acc[5] = {0.f, 0.f, 0.f, 0.f, 0.f};
#pragma unroll
      for (int p = 0; p < 4; p++) {
        const float wt = __builtin_amdgcn_exp2f((asf(qp[p][1]) - M) * LOG2E);
        acc[0] = fmaf(wt, lo16(qp[p][0]), acc[0]); acc[1] = fmaf(wt, hi16(qp[p][0]), acc[1]);
        acc[2] = fmaf(wt, lo16(qp[p][2]), acc[2]); acc[3] = fmaf(wt, hi16(qp[p][2]), acc[3]);
        acc[4] = fmaf(wt, asf(qp[p][3]), acc[4]);
      }
#pragma unroll
      for (int k = 0; k < 5; k++) { float t = acc[k]; t += dpp<ROR4>(t); t += dpp<ROR8>(t); acc[k] = sum32(sum16(t)); }
      const float inv = __builtin_amdgcn_rcpf(acc[4]);
      if (l < 4) *(LAS v2u*)(L + L_X3 + 32 * w + 8 * l) = v2u{pk(acc[0] * inv, acc[1] * inv), pk(acc[2] * inv, acc[3] * inv)};
    }
    lds_sig<4 * C_X3>();
    STAMP(7);
    STAMP_OUT();
  } else {
    const int w = wave, a = l >> 3, ch = l & 7;
    const rsrc_t rx = mkrs(px, 2048), rq = mkrs(pwq, 8388608);
    const unsigned row0 = 256 * r + 64 * w + 8 * a;
    const v4u xa = ld16<0>(rx, 32 * l, 0), xb = ld16<0>(rx, 32 * l + 16, 0), xf = ld16<0>(rx, 2 * row0, 0);
    v4u ring[8];
    {
      const unsigned vo = row0 * 8192 + 128 * cbq + 16 * ch;
#pragma unroll
      for (int i = 0; i < 8; i++) ring[i] = ld16<AUX_NT>(rq, vo, i * 8192);
    }
    FENCE();
    unsigned E;
    asm volatile("s_load_dword %0, %1, 0x0" : "=s"(E) : "s"(ws) : "memory");
    STAMP(1);
    v4u X;
    {
      float xv[16];
#pragma unroll
      for (int k = 0; k < 4; k++) { xv[2 * k] = lo16(xa[k]); xv[2 * k + 1] = hi16(xa[k]); xv[8 + 2 * k] = lo16(xb[k]); xv[9 + 2 * k] = hi16(xb[k]); }
      float sm = 0.f;
#pragma unroll
      for (int k = 0; k < 16; k++) sm += xv[k];
      const float mean = wave_sum(sm) * (1.f / 1024.f);
      float sq = 0.f;
#pragma unroll
      for (int k = 0; k < 16; k++) { const float d = xv[k] - mean; sq = fmaf(d, d, sq); }
      const float var = wave_sum(sq) * (1.f / 1024.f);
      const float mb = rbf(mean), den = rbf(__builtin_amdgcn_sqrtf(rbf(rbf(var) + 1e-5f))), inv = __builtin_amdgcn_rcpf(den);
#pragma unroll
      for (int k = 0; k < 4; k++) { const unsigned c = pk(lo16(xf[k]) - mb, hi16(xf[k]) - mb); X[k] = pk(lo16(c) * inv, hi16(c) * inv); }
    }
    unsigned sel[4][2];
#pragma unroll
    for (int d = 0; d < 4; d++)
#pragma unroll
      for (int e = 0; e < 2; e++) {
        const unsigned h = e ? 0x0302u : 0x0100u;
        sel[d][e] = ((ch >> 1) == d) ? ((ch & 1) ? ((h << 16) | 0x0c0cu) : (0x0c0c0000u | h)) : 0x0c0c0c0cu;
      }
#define DIAG(Xf, i) v4u{__builtin_amdgcn_perm(0u, Xf[(i) >> 1], sel[0][(i) & 1]), __builtin_amdgcn_perm(0u, Xf[(i) >> 1], sel[1][(i) & 1]), \
                        __builtin_amdgcn_perm(0u, Xf[(i) >> 1], sel[2][(i) & 1]), __builtin_amdgcn_perm(0u, Xf[(i) >> 1], sel[3][(i) & 1])}
    v4u A[8];
#pragma unroll
    for (int i = 0; i < 8; i++) { A[i] = DIAG(X, i); PIN(A[i]); }
    FENCE();
    asm volatile("s_waitcnt lgkmcnt(0)" : "+s"(E), "+v"(pfx) :: "memory");
    const unsigned T = E + 0x01010101u;
    STAMP(2);
    const rsrc_t rk = mkrs(pk_, 67108864), rv = mkrs(pv, 67108864);
    const unsigned kvo = (unsigned)(g * 65536 + 61440 + 64 * s + 16 * w) * 256u + 16 * l;
    f4 d = {0.f, 0.f, 0.f, 0.f};
#pragma unroll
    for (int i = 0; i < 8; i++) {
      d = mfma(A[i], ring[i], d);
      FENCE();
      ring[i] = ld16<AUX_NT>(i < 4 ? rk : rv, kvo, (i & 3) * 1024);
      FENCE();
    }
    STAMP(3);
    __builtin_amdgcn_s_barrier();
    merge_publish<L_QM, C_QM>(L, d, w, l, rws, WS_Q + (unsigned)(((cbq >> 4) * 4 + r) * 16 + (cbq & 15)) * 256u, T);
    FENCE();
    STAMP(4);
    const rsrc_t ro = mkrs(pwo, 8388608), r1 = mkrs(pw1, 7340032), r2 = mkrs(pw2, 7340032);
    const unsigned oc = 512 * r + 128 * w + 16 * ch;
    const unsigned voo = (unsigned)(64 * cbo + 8 * a) * 2048u + oc;
    LAS char* kvw = L + L_K + (16 * w + (l >> 4)) * KROW + 16 * (l & 15);
    v4u wo[8];
    d = f4{0.f, 0.f, 0.f, 0.f};
    if (hasF) {
      const unsigned v1 = row0 * 7168 + 128 * fb + 16 * ch, v2 = (unsigned)(64 * fb2 + 8 * a) * 2048u + oc;
#pragma unroll
      for (int i = 0; i < 8; i++) {
        *(LAS v4u*)(kvw + (i >> 2) * (L_V - L_K) + (i & 3) * 4 * KROW) = ring[i];
        FENCE();
        ring[i] = ld16<AUX_NT>(r1, v1, i * 7168);
        FENCE();
      }
      lds_sig<4 * C_KV>();
      STAMP(5);
#pragma unroll
      for (int i = 0; i < 8; i++) {
        d = mfma(A[i], ring[i], d);
        FENCE();
        ring[i] = ld16<AUX_NT>(r2, v2, i * 2048);
        FENCE();
      }
      STAMP(6);
      if (merge_publish<L_HM, C_HM>(L, d, w, l, rws, WS_H + (unsigned)(fb * 4 + r) * 256u, T)) lds_sig<4 * C_HP>();
      FENCE();
#pragma unroll
      for (int i = 0; i < 8; i++) {
        PIN(ring[i]);
        FENCE();
        wo[i] = ld16<AUX_NT>(ro, voo, i * 2048);
        FENCE();
      }
      STAMP(7);
      lds_wait<4 * C_X2, 1, 0>();
      const v4u X2 = *(LAS v4u*)(L + L_X2 + 16 * a);
      d = f4{0.f, 0.f, 0.f, 0.f};
#pragma unroll
      for (int i = 0; i < 8; i++) d = mfma(DIAG(X2, i), ring[i], d);
      FENCE();
    } else {
#pragma unroll
      for (int i = 0; i < 8; i++) {
        *(LAS v4u*)(kvw + (i >> 2) * (L_V - L_K) + (i & 3) * 4 * KROW) = ring[i];
        FENCE();
        wo[i] = ld16<AUX_NT>(ro, voo, i * 2048);
        FENCE();
      }
      lds_sig<4 * C_KV>();
      STAMP(5);
    }
    STAMP(8);
    lds_wait<4 * C_X3, 4, 0>();
    STAMP(9);
    const v4u X3 = *(LAS v4u*)(L + L_X3 + 16 * a);
#pragma unroll
    for (int i = 0; i < 8; i++) d = mfma(DIAG(X3, i), wo[i], d);
    v4u o;
#pragma unroll
    for (int k = 0; k < 4; k++) {
      auto sw = __builtin_amdgcn_permlane32_swap(asu(d[k]), asu(d[k]), false, false);
      o[k] = tag8(asf(sw[0]) + dpp<ROR8>(asf(sw[1])), (T >> (8 * k)) & 0xffu);
    }
    if ((l & 0x28) == 0) st16(o, rws, WS_R + (unsigned)(((r * 4 + w) * 2 + (l >> 4)) * 64 + cb) * 128u + 16 * (l & 7));
    STAMP(10);
    STAMP_OUT();
  }
}

extern "C" void kernel_launch(void* const* d_in, const int* in_sizes, int n_in, void* d_out, int out_size, void* d_ws, size_t ws_size,
                              hipStream_t stream) {
  fused_decode<<<256, 576, 0, stream>>>(d_in[0], d_in[3], (unsigned*)d_ws, d_in[1], d_in[2], d_in[6], d_in[7], d_in[4], d_out);
}
```

```cpp
#include <hip/hip_runtime.h>

__global__ void __launch_bounds__(512, 2) __attribute__((aligned(4096)))
fused_decode(const void* p_x, unsigned n_x, unsigned f_x,
             const void* p_wq, unsigned n_wq, unsigned f_wq,
             const void* p_ws, unsigned n_ws, unsigned f_ws,
             const void* p_out, unsigned n_out, unsigned f_out,
             const void* p_kc, unsigned n_kc, unsigned f_kc,
             const void* p_vc, unsigned n_vc, unsigned f_vc,
             const void* p_w1, unsigned n_w1, unsigned f_w1,
             const void* p_w2, unsigned n_w2, unsigned f_w2,
             const void* p_wo, unsigned n_wo, unsigned f_wo) {
  __shared__ __attribute__((aligned(16))) char lds_[42576];
  asm volatile(
      ".Lk_begin:\n"
      "s_load_dwordx16 s[4:19], s[0:1], 0x0\n"
      "s_add_u32 s2, s2, 0xe0\n"
      "s_and_b32 s2, s2, 0xff\n"
      "s_getpc_b64 s[34:35]\n"
      "v_subrev_u32 v1, 0x140, v0\n"
      "s_cmpk_lt_u32 s2, 16\n"
      "s_cselect_b32 s52, (.Lk_end-.Lk_begin)/128, 0\n"
      "v_cmp_gt_u32 vcc, s52, v1\n"
      "s_cbranch_vccz .Lk_nopf\n"
      "s_mov_b64 exec, vcc\n"
      "v_lshlrev_b32 v1, 7, v1\n"
      "global_load_dword v127, v1, s[34:35]\n"
      "s_mov_b64 exec, -1\n"
      ".Lk_nopf:\n"
      "v_readfirstlane_b32 s3, v0\n"
      "s_bfe_u32 s48, s2, 0x20003\n"
      "s_add_u32 s52, s2, 2\n"
      "s_and_b32 s52, s52, 7\n"
      "s_lshr_b32 s53, s2, 5\n"
      "s_xor_b32 s53, s53, 4\n"
      "s_lshl3_add_u32 s49, s52, s53\n"
      "s_lshl_b32 s52, s48, 21\n"
      "s_lshl_b32 s53, s49, 7\n"
      "s_or_b32 s52, s52, s53\n"
      "v_lshrrev_b32 v7, 3, v0\n"
      "v_and_b32 v6, 7, v0\n"
      "v_and_b32 v5, 63, v0\n"
      "v_lshl_add_u32 v2, v7, 16, s52\n"
      "v_lshlrev_b32 v4, 5, v5\n"
      "v_lshl_or_b32 v2, v6, 4, v2\n"
      "s_movk_i32 s40, 0x2000\n"
      "s_movk_i32 s41, 0x4000\n"
      "s_movk_i32 s42, 0x6000\n"
      "v_add_u32 v3, 0x8000, v2\n"
      "s_cmpk_lt_u32 s3, 0x100\n"
      "s_cbranch_scc1 .Lk_stream\n"
      "s_setprio 3\n"
      "s_cmpk_eq_u32 s3, 0x100\n"
      "s_cbranch_scc0 .Lk_nox0\n"
      "s_waitcnt lgkmcnt(0)\n"
      "buffer_load_dwordx4 v[8:11], v4, s[4:7], 0 offen\n"
      "buffer_load_dwordx4 v[12:15], v4, s[4:7], 0 offen offset:16\n"
      "buffer_load_dword v40, off, s[12:15], 0 sc1\n"
      ".Lk_nox0:\n"
      "s_barrier\n"
      "s_memrealtime s[66:67]\n"
      "s_branch .Lk_comm\n"
      ".Lk_stream:\n"
      "s_waitcnt lgkmcnt(0)\n"
      "s_barrier\n"
      "buffer_load_dwordx4 v[80:83], v2, s[8:11], 0 offen nt\n"
      "buffer_load_dwordx4 v[84:87], v2, s[8:11], s40 offen nt\n"
      "buffer_load_dwordx4 v[88:91], v2, s[8:11], s41 offen nt\n"
      "buffer_load_dwordx4 v[92:95], v2, s[8:11], s42 offen nt\n"
      "buffer_load_dwordx4 v[96:99], v3, s[8:11], 0 offen nt\n"
      "buffer_load_dwordx4 v[100:103], v3, s[8:11], s40 offen nt\n"
      "buffer_load_dwordx4 v[104:107], v3, s[8:11], s41 offen nt\n"
      "buffer_load_dwordx4 v[108:111], v3, s[8:11], s42 offen nt\n"
      "s_memrealtime s[66:67]\n"
      "s_load_dwordx8 s[20:27], s[0:1], 0x40\n"
      "s_load_dwordx4 s[28:31], s[0:1], 0x60\n"
      "s_load_dwordx8 s[68:75], s[0:1], 0x70\n"
      "s_movk_i32 s43, 0x1000\n"
      "s_movk_i32 s44, 0x3000\n"
      "s_lshr_b32 s45, s2, 3\n"
      "s_and_b32 s47, s2, 1\n"
      "s_bfe_u32 s46, s2, 0x20001\n"
      "s_bfe_u32 s50, s3, 0x20006\n"
      "s_mov_b32 s39, 0x800000\n"
      "s_mov_b32 s51, 0x01010101\n"
      "s_mov_b32 s56, 0x00ff00ff\n"
      "s_mov_b32 s57, 0x00ff00ff\n"
      "s_lshl_b32 s52, s46, 5\n"
      "s_add_u32 s59, s52, s45\n"
      "v_lshlrev_b32 v1, 4, v5\n"
      "v_bfe_u32 v7, v0, 3, 3\n"
      "s_lshl_b32 s52, s47, 5\n"
      "s_add_u32 s52, s52, s45\n"
      "s_lshl2_add_u32 s52, s52, s50\n"
      "s_lshl_b32 s52, s52, 12\n"
      "s_lshl_b32 s53, s46, 24\n"
      "s_add_u32 s52, s52, s53\n"
      "s_add_u32 s52, s52, 0xf00000\n"
      "v_add_u32 v8, s52, v1\n"
      "s_mul_i32 s52, s50, 4352\n"
      "v_lshrrev_b32 v23, 4, v5\n"
      "v_lshl_add_u32 v21, v23, 4, v1\n"
      "v_add_u32 v12, s52, v21\n"
      "s_add_u32 s52, s2, 4\n"
      "s_and_b32 s52, s52, 7\n"
      "s_mul_i32 s52, s52, 28\n"
      "s_add_u32 s52, s52, s45\n"
      "s_and_b32 s58, s52, 15\n"
      "s_lshr_b32 s52, s52, 4\n"
      "s_lshl2_add_u32 s53, s52, s50\n"
      "s_mul_i32 s52, s58, 458752\n"
      "s_lshl_b32 s54, s53, 7\n"
      "s_add_u32 s52, s52, s54\n"
      "v_lshlrev_b32 v4, 4, v6\n"
      "v_mul_u32_u24 v21, 57344, v7\n"
      "v_add3_u32 v9, v21, v4, s52\n"
      "s_lshl4_add_u32 s53, s53, s58\n"
      "s_lshl_b32 s53, s53, 8\n"
      "s_add_u32 s53, s53, 69632\n"
      "v_lshl_add_u32 v21, v23, 7, v4\n"
      "v_add_u32 v15, s53, v21\n"
      "s_lshl2_add_u32 s55, s47, s50\n"
      "s_lshl_b32 s54, s55, 8\n"
      "s_mul_i32 s52, s59, 57344\n"
      "s_add_u32 s52, s52, s54\n"
      "v_lshl_add_u32 v21, v7, 13, v4\n"
      "v_add_u32 v10, s52, v21\n"
      "v_cmp_gt_u32 vcc, 7, v7\n"
      "v_bfrev_b32 v21, 2\n"
      "s_xor_b32 s52, s59, 64\n"
      "v_cndmask_b32 v10, v21, v10, vcc\n"
      "s_lshr_b32 s52, s52, 4\n"
      "s_lshl_b32 s52, s52, 20\n"
      "s_and_b32 s53, s45, 15\n"
      "s_lshl_b32 s53, s53, 14\n"
      "s_add_u32 s52, s52, s53\n"
      "s_add_u32 s52, s52, s54\n"
      "v_lshl_add_u32 v21, v23, 18, v4\n"
      "v_and_b32 v11, 8, v5\n"
      "v_lshl_add_u32 v21, v11, 10, v21\n"
      "v_add_u32 v11, s52, v21\n"
      "s_lshl_b32 s52, s55, 16\n"
      "s_lshl_b32 s53, s59, 6\n"
      "s_add_u32 s52, s52, s53\n"
      "s_add_u32 s52, s52, 1347584\n"
      "v_lshl_add_u32 v16, v23, 14, s52\n"
      "v_and_b32 v21, 4, v5\n"
      "v_lshl_add_u32 v16, v21, 11, v16\n"
      "v_and_b32 v21, 48, v1\n"
      "v_add_u32 v16, v21, v16\n"
      "s_lshr_b32 s52, s49, 4\n"
      "s_lshl2_add_u32 s52, s52, s48\n"
      "s_and_b32 s53, s49, 15\n"
      "s_lshl4_add_u32 s52, s52, s53\n"
      "s_lshl_b32 s52, s52, 8\n"
      "s_add_u32 s52, s52, 4096\n"
      "v_add_u32 v14, s52, v1\n"
      "s_lshl_b32 s52, s50, 9\n"
      "v_and_b32 v21, 15, v5\n"
      "v_lshl_add_u32 v13, v21, 5, s52\n"
      "v_and_or_b32 v13, v5, 16, v13\n"
      "v_lshlrev_b32 v17, 4, v7\n"
      "v_mov_b32 v18, 1\n"
      "v_mov_b32 v19, s50\n"
      "v_mov_b32 v20, 0\n"
      "v_mov_b32 v22, 0xffffff00\n"
      "v_and_b32 v2, 1, v6\n"
      "v_cmp_eq_u32 vcc, 0, v2\n"
      "v_mov_b32 v2, 0x01000c0c\n"
      "v_mov_b32 v3, 0x0c0c0100\n"
      "v_cndmask_b32 v2, v2, v3, vcc\n"
      "v_mov_b32 v3, 0x03020c0c\n"
      "v_mov_b32 v21, 0x0c0c0302\n"
      "v_cndmask_b32 v3, v3, v21, vcc\n"
      "v_mov_b32 v21, 0x0c0c0c0c\n"
      "s_mov_b32 s60, 0x03030303\n"
      "s_mov_b32 s61, 0x03030303\n"
      "v_cndmask_b32 v24, v21, v2, s[60:61]\n"
      "v_cndmask_b32 v28, v21, v3, s[60:61]\n"
      "s_lshl_b64 s[60:61], s[60:61], 2\n"
      "v_cndmask_b32 v25, v21, v2, s[60:61]\n"
      "v_cndmask_b32 v29, v21, v3, s[60:61]\n"
      "s_lshl_b64 s[60:61], s[60:61], 2\n"
      "v_cndmask_b32 v26, v21, v2, s[60:61]\n"
      "v_cndmask_b32 v30, v21, v3, s[60:61]\n"
      "s_lshl_b64 s[60:61], s[60:61], 2\n"
      "v_cndmask_b32 v27, v21, v2, s[60:61]\n"
      "v_cndmask_b32 v31, v21, v3, s[60:61]\n"
      "s_lshl2_add_u32 s52, s48, s50\n"
      "s_lshl_b32 s52, s52, 7\n"
      "v_add_u32 v2, s52, v17\n"
      "s_lshl_b32 s52, s58, 7\n"
      "v_add_u32 v3, s52, v17\n"
      "s_mov_b32 s60, 0xff00ff\n"
      "s_mov_b32 s61, 0xff00ff00\n"
      "s_mov_b32 s55, 0\n"
      "s_waitcnt lgkmcnt(0)\n"
      "buffer_load_dwordx4 v[112:115], v8, s[20:23], 0 offen nt\n"
      "buffer_load_dwordx4 v[116:119], v8, s[20:23], 0 offen offset:1024 nt\n"
      "s_lshl_b32 s66, s66, 4\n"
      "s_add_u32 s66, s66, 4040\n"
      "s_barrier\n"
      "ds_read_b128 v[32:35], v2 offset:80\n"
      "ds_read_b128 v[36:39], v3 offset:80\n"
      "ds_read_b32 v21, v20 offset:64\n"
      "s_waitcnt lgkmcnt(0)\n"
      "v_readfirstlane_b32 s34, v21\n"
      "v_perm_b32 v48, v32, v32, v24\n"
      "v_perm_b32 v49, v32, v32, v25\n"
      "v_perm_b32 v50, v32, v32, v26\n"
      "v_perm_b32 v51, v32, v32, v27\n"
      "v_perm_b32 v52, v32, v32, v28\n"
      "v_perm_b32 v53, v32, v32, v29\n"
      "v_perm_b32 v54, v32, v32, v30\n"
      "v_perm_b32 v55, v32, v32, v31\n"
      "v_perm_b32 v56, v33, v33, v24\n"
      "v_perm_b32 v57, v33, v33, v25\n"
      "v_perm_b32 v58, v33, v33, v26\n"
      "v_perm_b32 v59, v33, v33, v27\n"
      "v_perm_b32 v60, v33, v33, v28\n"
      "v_perm_b32 v61, v33, v33, v29\n"
      "v_perm_b32 v62, v33, v33, v30\n"
      "v_perm_b32 v63, v33, v33, v31\n"
      "v_perm_b32 v64, v34, v34, v24\n"
      "v_perm_b32 v65, v34, v34, v25\n"
      "v_perm_b32 v66, v34, v34, v26\n"
      "v_perm_b32 v67, v34, v34, v27\n"
      "v_perm_b32 v68, v34, v34, v28\n"
      "v_perm_b32 v69, v34, v34, v29\n"
      "v_perm_b32 v70, v34, v34, v30\n"
      "v_perm_b32 v71, v34, v34, v31\n"
      "v_perm_b32 v72, v35, v35, v24\n"
      "v_perm_b32 v73, v35, v35, v25\n"
      "v_perm_b32 v74, v35, v35, v26\n"
      "v_perm_b32 v75, v35, v35, v27\n"
      "v_perm_b32 v76, v35, v35, v28\n"
      "v_perm_b32 v77, v35, v35, v29\n"
      "v_perm_b32 v78, v35, v35, v30\n"
      "v_perm_b32 v79, v35, v35, v31\n"
      "s_add_u32 s34, s34, s51\n"
      "s_and_b32 s35, s34, 0xff\n"
      "s_bfe_u32 s36, s34, 0x80008\n"
      "s_bfe_u32 s37, s34, 0x80010\n"
      "s_lshr_b32 s38, s34, 24\n"
      "s_waitcnt vmcnt(9)\n"
      "v_mfma_f32_16x16x32_bf16 v[40:43], v[48:51], v[80:83], 0\n"
      "s_waitcnt vmcnt(8)\n"
      "v_mfma_f32_16x16x32_bf16 v[40:43], v[52:55], v[84:87], v[40:43]\n"
      "s_waitcnt vmcnt(7)\n"
      "v_mfma_f32_16x16x32_bf16 v[40:43], v[56:59], v[88:91], v[40:43]\n"
      "s_waitcnt vmcnt(6)\n"
      "v_mfma_f32_16x16x32_bf16 v[40:43], v[60:63], v[92:95], v[40:43]\n"
      "s_waitcnt vmcnt(5)\n"
      "v_mfma_f32_16x16x32_bf16 v[40:43], v[64:67], v[96:99], v[40:43]\n"
      "s_waitcnt vmcnt(4)\n"
      "v_mfma_f32_16x16x32_bf16 v[40:43], v[68:71], v[100:103], v[40:43]\n"
      "s_waitcnt vmcnt(3)\n"
      "v_mfma_f32_16x16x32_bf16 v[40:43], v[72:75], v[104:107], v[40:43]\n"
      "s_waitcnt vmcnt(2)\n"
      "v_mfma_f32_16x16x32_bf16 v[40:43], v[76:79], v[108:111], v[40:43]\n"
      "buffer_load_dwordx4 v[120:123], v8, s[20:23], 0 offen offset:2048 nt\n"
      "buffer_load_dwordx4 v[124:127], v8, s[20:23], 0 offen offset:3072 nt\n"
      "buffer_load_dwordx4 v[128:131], v8, s[24:27], 0 offen nt\n"
      "buffer_load_dwordx4 v[132:135], v8, s[24:27], 0 offen offset:1024 nt\n"
      "s_mov_b64 exec, s[60:61]\n"
      "s_nop 2\n"
      "ds_write_b128 v13, v[40:43] offset:2128\n"
      "s_mov_b64 exec, 1\n"
      "ds_add_rtn_u32 v21, v20, v18 offset:0\n"
      "s_mov_b64 exec, 0xffff\n"
      "ds_read_b128 v[80:83], v1 offset:2128\n"
      "ds_read_b128 v[84:87], v1 offset:2384\n"
      "ds_read_b128 v[88:91], v1 offset:2640\n"
      "ds_read_b128 v[92:95], v1 offset:2896\n"
      "ds_read_b128 v[96:99], v1 offset:3152\n"
      "ds_read_b128 v[100:103], v1 offset:3408\n"
      "ds_read_b128 v[104:107], v1 offset:3664\n"
      "ds_read_b128 v[108:111], v1 offset:3920\n"
      "s_waitcnt lgkmcnt(8)\n"
      "v_readfirstlane_b32 s52, v21\n"
      "s_cmp_eq_u32 s52, 3\n"
      "s_cbranch_scc0 .Lk_qm\n"
      "s_waitcnt lgkmcnt(6)\n"
      "v_pk_add_f32 v[80:81], v[80:81], v[84:85]\n"
      "v_pk_add_f32 v[82:83], v[82:83], v[86:87]\n"
      "s_waitcnt lgkmcnt(4)\n"
      "v_pk_add_f32 v[88:89], v[88:89], v[92:93]\n"
      "v_pk_add_f32 v[90:91], v[90:91], v[94:95]\n"
      "v_pk_add_f32 v[80:81], v[80:81], v[88:89]\n"
      "v_pk_add_f32 v[82:83], v[82:83], v[90:91]\n"
      "s_waitcnt lgkmcnt(2)\n"
      "v_pk_add_f32 v[96:97], v[96:97], v[100:101]\n"
      "v_pk_add_f32 v[98:99], v[98:99], v[102:103]\n"
      "s_waitcnt lgkmcnt(0)\n"
      "v_pk_add_f32 v[104:105], v[104:105], v[108:109]\n"
      "v_pk_add_f32 v[106:107], v[106:107], v[110:111]\n"
      "v_pk_add_f32 v[96:97], v[96:97], v[104:105]\n"
      "v_pk_add_f32 v[98:99], v[98:99], v[106:107]\n"
      "v_pk_add_f32 v[80:81], v[80:81], v[96:97]\n"
      "v_pk_add_f32 v[82:83], v[82:83], v[98:99]\n"
      "v_mov_b32 v21, s51\n"
      "v_add_u32 v80, 32, v80\n"
      "v_add_u32 v81, 32, v81\n"
      "v_add_u32 v82, 32, v82\n"
      "v_add_u32 v83, 32, v83\n"
      "v_bfi_b32 v80, 63, s35, v80\n"
      "v_bfi_b32 v81, 63, s36, v81\n"
      "v_bfi_b32 v82, 63, s37, v82\n"
      "v_bfi_b32 v83, 63, s38, v83\n"
      "buffer_store_dwordx4 v[80:83], v14, s[12:15], 0 offen sc1\n"
      "ds_write_b32 v20, v21 offset:32\n"
      ".Lk_qm:\n"
      "s_mov_b64 exec, -1\n"
      ".Lk_w_1:\n"
      "ds_read_b32 v21, v20 offset:32\n"
      "s_memrealtime s[64:65]\n"
      "s_sub_u32 s39, s39, 1\n"
      "s_cbranch_scc1 .Lk_exit\n"
      "s_waitcnt lgkmcnt(0)\n"
      "v_cmp_ne_u32 vcc, s51, v21\n"
      "s_cbranch_vccnz .Lk_w_1\n"
      "s_lshl_b32 s64, s64, 4\n"
      "s_sub_u32 s64, s64, s66\n"
      "s_cmpk_lt_i32 s64, -2120\n"
      "s_cselect_b32 s64, 0x100000, 0\n"
      "s_sub_u32 s66, s66, s64\n"
      "s_cmpk_lt_u32 s2, 0xe0\n"
      "s_cbranch_scc0 .Lk_spare\n"
      "buffer_load_dwordx4 v[136:139], v8, s[24:27], 0 offen offset:2048 nt\n"
      "buffer_load_dwordx4 v[140:143], v8, s[24:27], 0 offen offset:3072 nt\n"
      "v_perm_b32 v48, v36, v36, v24\n"
      "v_perm_b32 v49, v36, v36, v25\n"
      "v_perm_b32 v50, v36, v36, v26\n"
      "v_perm_b32 v51, v36, v36, v27\n"
      "v_perm_b32 v52, v36, v36, v28\n"
      "v_perm_b32 v53, v36, v36, v29\n"
      "v_perm_b32 v54, v36, v36, v30\n"
      "v_perm_b32 v55, v36, v36, v31\n"
      "v_perm_b32 v56, v37, v37, v24\n"
      "v_perm_b32 v57, v37, v37, v25\n"
      "v_perm_b32 v58, v37, v37, v26\n"
      "v_perm_b32 v59, v37, v37, v27\n"
      "v_perm_b32 v60, v37, v37, v28\n"
      "v_perm_b32 v61, v37, v37, v29\n"
      "v_perm_b32 v62, v37, v37, v30\n"
      "v_perm_b32 v63, v37, v37, v31\n"
      "v_perm_b32 v64, v38, v38, v24\n"
      "v_perm_b32 v65, v38, v38, v25\n"
      "v_perm_b32 v66, v38, v38, v26\n"
      "v_perm_b32 v67, v38, v38, v27\n"
      "v_perm_b32 v68, v38, v38, v28\n"
      "v_perm_b32 v69, v38, v38, v29\n"
      "v_perm_b32 v70, v38, v38, v30\n"
      "v_perm_b32 v71, v38, v38, v31\n"
      "v_perm_b32 v72, v39, v39, v24\n"
      "v_perm_b32 v73, v39, v39, v25\n"
      "v_perm_b32 v74, v39, v39, v26\n"
      "v_perm_b32 v75, v39, v39, v27\n"
      "v_perm_b32 v76, v39, v39, v28\n"
      "v_perm_b32 v77, v39, v39, v29\n"
      "v_perm_b32 v78, v39, v39, v30\n"
      "v_perm_b32 v79, v39, v39, v31\n"
      "s_waitcnt vmcnt(7)\n"
      "ds_write_b128 v12, v[112:115] offset:7760\n"
      "s_waitcnt vmcnt(6)\n"
      "ds_write_b128 v12, v[116:119] offset:8848\n"
      "s_waitcnt vmcnt(5)\n"
      "ds_write_b128 v12, v[120:123] offset:9936\n"
      "buffer_load_dwordx4 v[144:147], v9, s[28:31], s55 offen nt\n"
      "s_add_u32 s55, s55, 7168\n"
      "s_waitcnt vmcnt(5)\n"
      "ds_write_b128 v12, v[124:127] offset:11024\n"
      "buffer_load_dwordx4 v[148:151], v9, s[28:31], s55 offen nt\n"
      "s_add_u32 s55, s55, 7168\n"
      "s_waitcnt vmcnt(5)\n"
      "ds_write_b128 v12, v[128:131] offset:25168\n"
      "buffer_load_dwordx4 v[152:155], v9, s[28:31], s55 offen nt\n"
      "s_add_u32 s55, s55, 7168\n"
      "s_waitcnt vmcnt(5)\n"
      "ds_write_b128 v12, v[132:135] offset:26256\n"
      "buffer_load_dwordx4 v[156:159], v9, s[28:31], s55 offen nt\n"
      "s_add_u32 s55, s55, 7168\n"
      "s_waitcnt vmcnt(5)\n"
      "ds_write_b128 v12, v[136:139] offset:27344\n"
      "buffer_load_dwordx4 v[160:163], v9, s[28:31], s55 offen nt\n"
      "s_add_u32 s55, s55, 7168\n"
      "s_waitcnt vmcnt(5)\n"
      "ds_write_b128 v12, v[140:143] offset:28432\n"
      "buffer_load_dwordx4 v[164:167], v9, s[28:31], s55 offen nt\n"
      "s_add_u32 s55, s55, 7168\n"
      "ds_write_b8 v19, v18 offset:8\n"
      "s_waitcnt vmcnt(5)\n"
      "v_mfma_f32_16x16x32_bf16 v[40:43], v[48:51], v[144:147], 0\n"
      "s_call_b64 s[62:63], .Lk_tick\n"
      "buffer_load_dwordx4 v[168:171], v9, s[28:31], s55 offen nt\n"
      "s_add_u32 s55, s55, 7168\n"
      "s_waitcnt vmcnt(5)\n"
      "v_mfma_f32_16x16x32_bf16 v[40:43], v[52:55], v[148:151], v[40:43]\n"
      "s_call_b64 s[62:63], .Lk_tick\n"
      "buffer_load_dwordx4 v[172:175], v9, s[28:31], s55 offen nt\n"
      "s_waitcnt vmcnt(5)\n"
      "v_mfma_f32_16x16x32_bf16 v[40:43], v[56:59], v[152:155], v[40:43]\n"
      "s_call_b64 s[62:63], .Lk_tick\n"
      "buffer_load_dwordx4 v[176:179], v10, s[68:71], 0 offen nt\n"
      "s_waitcnt vmcnt(5)\n"
      "v_mfma_f32_16x16x32_bf16 v[40:43], v[60:63], v[156:159], v[40:43]\n"
      "s_call_b64 s[62:63], .Lk_tick\n"
      "buffer_load_dwordx4 v[180:183], v10, s[68:71], 0 offen offset:128 nt\n"
      "ds_write_b8 v19, v18 offset:4\n"
      "s_waitcnt vmcnt(5)\n"
      "v_mfma_f32_16x16x32_bf16 v[40:43], v[64:67], v[160:163], v[40:43]\n"
      "s_call_b64 s[62:63], .Lk_tick\n"
      "buffer_load_dwordx4 v[184:187], v10, s[68:71], 0 offen offset:2048 nt\n"
      "s_waitcnt vmcnt(5)\n"
      "v_mfma_f32_16x16x32_bf16 v[40:43], v[68:71], v[164:167], v[40:43]\n"
      "s_call_b64 s[62:63], .Lk_tick\n"
      "buffer_load_dwordx4 v[188:191], v10, s[68:71], 0 offen offset:2176 nt\n"
      "s_waitcnt vmcnt(5)\n"
      "v_mfma_f32_16x16x32_bf16 v[40:43], v[72:75], v[168:171], v[40:43]\n"
      "s_call_b64 s[62:63], .Lk_tick\n"
      "buffer_load_dwordx4 v[192:195], v10, s[68:71], s43 offen nt\n"
      "s_waitcnt vmcnt(5)\n"
      "v_mfma_f32_16x16x32_bf16 v[40:43], v[76:79], v[172:175], v[40:43]\n"
      "s_call_b64 s[62:63], .Lk_tick\n"
      "buffer_load_dwordx4 v[196:199], v10, s[68:71], s43 offen offset:128 nt\n"
      "s_nop 5\n"
      "v_mov_b32 v44, v40\n"
      "v_mov_b32 v45, v41\n"
      "v_mov_b32 v46, v42\n"
      "v_mov_b32 v47, v43\n"
      "v_permlane32_swap_b32 v40, v44\n"
      "v_permlane32_swap_b32 v41, v45\n"
      "v_permlane32_swap_b32 v42, v46\n"
      "v_permlane32_swap_b32 v43, v47\n"
      "v_add_f32_dpp v40, v44, v40 row_ror:8 row_mask:0xf bank_mask:0xf bound_ctrl:1\n"
      "v_add_f32_dpp v41, v45, v41 row_ror:8 row_mask:0xf bank_mask:0xf bound_ctrl:1\n"
      "v_add_f32_dpp v42, v46, v42 row_ror:8 row_mask:0xf bank_mask:0xf bound_ctrl:1\n"
      "v_add_f32_dpp v43, v47, v43 row_ror:8 row_mask:0xf bank_mask:0xf bound_ctrl:1\n"
      "s_mov_b64 exec, 0xff00ff\n"
      "v_and_or_b32 v40, v40, v22, s35\n"
      "v_and_or_b32 v41, v41, v22, s36\n"
      "v_and_or_b32 v42, v42, v22, s37\n"
      "v_and_or_b32 v43, v43, v22, s38\n"
      "buffer_store_dwordx4 v[40:43], v15, s[12:15], 0 offen sc1\n"
      "s_mov_b64 exec, -1\n"
      ".Lk_pace:\n"
      "ds_read_b32 v21, v20 offset:36\n"
      "s_waitcnt vmcnt(5)\n"
      "s_waitcnt lgkmcnt(0)\n"
      "v_cmp_eq_u32 vcc, s51, v21\n"
      "s_cbranch_vccnz .Lk_burst6\n"
      "s_call_b64 s[62:63], .Lk_tick\n"
      "buffer_load_dwordx4 v[200:203], v10, s[68:71], s43 offen offset:2048 nt\n"
      "ds_read_b32 v21, v20 offset:36\n"
      "s_waitcnt vmcnt(5)\n"
      "s_waitcnt lgkmcnt(0)\n"
      "v_cmp_eq_u32 vcc, s51, v21\n"
      "s_cbranch_vccnz .Lk_burst7\n"
      "s_call_b64 s[62:63], .Lk_tick\n"
      "buffer_load_dwordx4 v[204:207], v10, s[68:71], s43 offen offset:2176 nt\n"
      "ds_read_b32 v21, v20 offset:36\n"
      "s_waitcnt vmcnt(5)\n"
      "s_waitcnt lgkmcnt(0)\n"
      "v_cmp_eq_u32 vcc, s51, v21\n"
      "s_cbranch_vccnz .Lk_burst8\n"
      "s_call_b64 s[62:63], .Lk_tick\n"
      "buffer_load_dwordx4 v[208:211], v11, s[72:75], 0 offen nt\n"
      "ds_read_b32 v21, v20 offset:36\n"
      "s_waitcnt vmcnt(5)\n"
      "s_waitcnt lgkmcnt(0)\n"
      "v_cmp_eq_u32 vcc, s51, v21\n"
      "s_cbranch_vccnz .Lk_burst9\n"
      "s_call_b64 s[62:63], .Lk_tick\n"
      "buffer_load_dwordx4 v[212:215], v11, s[72:75], 0 offen offset:128 nt\n"
      "ds_read_b32 v21, v20 offset:36\n"
      "s_waitcnt vmcnt(5)\n"
      "s_waitcnt lgkmcnt(0)\n"
      "v_cmp_eq_u32 vcc, s51, v21\n"
      "s_cbranch_vccnz .Lk_burst10\n"
      "s_call_b64 s[62:63], .Lk_tick\n"
      "buffer_load_dwordx4 v[216:219], v11, s[72:75], 0 offen offset:2048 nt\n"
      "ds_read_b32 v21, v20 offset:36\n"
      "s_waitcnt vmcnt(5)\n"
      "s_waitcnt lgkmcnt(0)\n"
      "v_cmp_eq_u32 vcc, s51, v21\n"
      "s_cbranch_vccnz .Lk_burst11\n"
      "s_call_b64 s[62:63], .Lk_tick\n"
      "buffer_load_dwordx4 v[220:223], v11, s[72:75], 0 offen offset:2176 nt\n"
      "ds_read_b32 v21, v20 offset:36\n"
      "s_waitcnt vmcnt(5)\n"
      "s_waitcnt lgkmcnt(0)\n"
      "v_cmp_eq_u32 vcc, s51, v21\n"
      "s_cbranch_vccnz .Lk_burst12\n"
      "s_call_b64 s[62:63], .Lk_tick\n"
      "buffer_load_dwordx4 v[224:227], v11, s[72:75], s43 offen nt\n"
      "ds_read_b32 v21, v20 offset:36\n"
      "s_waitcnt vmcnt(5)\n"
      "s_waitcnt lgkmcnt(0)\n"
      "v_cmp_eq_u32 vcc, s51, v21\n"
      "s_cbranch_vccnz .Lk_burst13\n"
      "s_call_b64 s[62:63], .Lk_tick\n"
      "buffer_load_dwordx4 v[228:231], v11, s[72:75], s43 offen offset:128 nt\n"
      "ds_read_b32 v21, v20 offset:36\n"
      "s_waitcnt vmcnt(5)\n"
      "s_waitcnt lgkmcnt(0)\n"
      "v_cmp_eq_u32 vcc, s51, v21\n"
      "s_cbranch_vccnz .Lk_burst14\n"
      "s_call_b64 s[62:63], .Lk_tick\n"
      "buffer_load_dwordx4 v[232:235], v11, s[72:75], s43 offen offset:2048 nt\n"
      "s_waitcnt vmcnt(5)\n"
      "s_call_b64 s[62:63], .Lk_tick\n"
      "buffer_load_dwordx4 v[236:239], v11, s[72:75], s43 offen offset:2176 nt\n"
      ".Lk_last:\n"
      ".Lk_w_3:\n"
      "ds_read_b32 v21, v20 offset:20\n"
      "ds_read_b64 v[32:33], v17 offset:7632\n"
      "s_sub_u32 s39, s39, 1\n"
      "s_cbranch_scc1 .Lk_exit\n"
      "s_waitcnt lgkmcnt(0)\n"
      "v_cmp_ne_u32 vcc, s51, v21\n"
      "s_cbranch_vccnz .Lk_w_3\n"
      "v_perm_b32 v48, v32, v32, v24\n"
      "v_perm_b32 v49, v32, v32, v25\n"
      "v_perm_b32 v50, v32, v32, v26\n"
      "v_perm_b32 v51, v32, v32, v27\n"
      "v_perm_b32 v52, v32, v32, v28\n"
      "v_perm_b32 v53, v32, v32, v29\n"
      "v_perm_b32 v54, v32, v32, v30\n"
      "v_perm_b32 v55, v32, v32, v31\n"
      "s_waitcnt vmcnt(8)\n"
      "v_mfma_f32_16x16x32_bf16 v[40:43], v[48:51], v[176:179], 0\n"
      "v_mfma_f32_16x16x32_bf16 v[44:47], v[48:51], v[180:183], 0\n"
      "v_perm_b32 v56, v33, v33, v24\n"
      "v_perm_b32 v57, v33, v33, v25\n"
      "v_perm_b32 v58, v33, v33, v26\n"
      "v_perm_b32 v59, v33, v33, v27\n"
      "v_mfma_f32_16x16x32_bf16 v[40:43], v[52:55], v[184:187], v[40:43]\n"
      "v_mfma_f32_16x16x32_bf16 v[44:47], v[52:55], v[188:191], v[44:47]\n"
      "v_perm_b32 v60, v33, v33, v28\n"
      "v_perm_b32 v61, v33, v33, v29\n"
      "v_perm_b32 v62, v33, v33, v30\n"
      "v_perm_b32 v63, v33, v33, v31\n"
      "v_mfma_f32_16x16x32_bf16 v[40:43], v[56:59], v[192:195], v[40:43]\n"
      "v_mfma_f32_16x16x32_bf16 v[44:47], v[56:59], v[196:199], v[44:47]\n"
      "v_mfma_f32_16x16x32_bf16 v[40:43], v[60:63], v[200:203], v[40:43]\n"
      "v_mfma_f32_16x16x32_bf16 v[44:47], v[60:63], v[204:207], v[44:47]\n"
      ".Lk_w_5:\n"
      "ds_read_b32 v21, v20 offset:24\n"
      "ds_read_b64 v[34:35], v17 offset:7640\n"
      "s_sub_u32 s39, s39, 1\n"
      "s_cbranch_scc1 .Lk_exit\n"
      "s_waitcnt lgkmcnt(0)\n"
      "v_cmp_ne_u32 vcc, s51, v21\n"
      "s_cbranch_vccnz .Lk_w_5\n"
      "v_perm_b32 v64, v34, v34, v24\n"
      "v_perm_b32 v65, v34, v34, v25\n"
      "v_perm_b32 v66, v34, v34, v26\n"
      "v_perm_b32 v67, v34, v34, v27\n"
      "v_perm_b32 v68, v34, v34, v28\n"
      "v_perm_b32 v69, v34, v34, v29\n"
      "v_perm_b32 v70, v34, v34, v30\n"
      "v_perm_b32 v71, v34, v34, v31\n"
      "s_waitcnt vmcnt(4)\n"
      "v_mfma_f32_16x16x32_bf16 v[40:43], v[64:67], v[208:211], v[40:43]\n"
      "v_mfma_f32_16x16x32_bf16 v[44:47], v[64:67], v[212:215], v[44:47]\n"
      "v_perm_b32 v72, v35, v35, v24\n"
      "v_perm_b32 v73, v35, v35, v25\n"
      "v_perm_b32 v74, v35, v35, v26\n"
      "v_perm_b32 v75, v35, v35, v27\n"
      "v_mfma_f32_16x16x32_bf16 v[40:43], v[68:71], v[216:219], v[40:43]\n"
      "v_mfma_f32_16x16x32_bf16 v[44:47], v[68:71], v[220:223], v[44:47]\n"
      "v_perm_b32 v76, v35, v35, v28\n"
      "v_perm_b32 v77, v35, v35, v29\n"
      "v_perm_b32 v78, v35, v35, v30\n"
      "v_perm_b32 v79, v35, v35, v31\n"
      "s_waitcnt vmcnt(2)\n"
      "v_mfma_f32_16x16x32_bf16 v[40:43], v[72:75], v[224:227], v[40:43]\n"
      "v_mfma_f32_16x16x32_bf16 v[44:47], v[72:75], v[228:231], v[44:47]\n"
      "s_waitcnt vmcnt(0)\n"
      "v_mfma_f32_16x16x32_bf16 v[40:43], v[76:79], v[232:235], v[40:43]\n"
      "v_mfma_f32_16x16x32_bf16 v[44:47], v[76:79], v[236:239], v[44:47]\n"
      "s_nop 7\n"
      "v_permlane32_swap_b32 v40, v44\n"
      "v_permlane32_swap_b32 v41, v45\n"
      "v_permlane32_swap_b32 v42, v46\n"
      "v_permlane32_swap_b32 v43, v47\n"
      "v_add_f32_dpp v40, v44, v40 row_ror:8 row_mask:0xf bank_mask:0xf bound_ctrl:1\n"
      "v_add_f32_dpp v41, v45, v41 row_ror:8 row_mask:0xf bank_mask:0xf bound_ctrl:1\n"
      "v_add_f32_dpp v42, v46, v42 row_ror:8 row_mask:0xf bank_mask:0xf bound_ctrl:1\n"
      "v_add_f32_dpp v43, v47, v43 row_ror:8 row_mask:0xf bank_mask:0xf bound_ctrl:1\n"
      "s_mov_b64 exec, s[56:57]\n"
      "v_and_or_b32 v40, v40, v22, s35\n"
      "v_and_or_b32 v41, v41, v22, s36\n"
      "v_and_or_b32 v42, v42, v22, s37\n"
      "v_and_or_b32 v43, v43, v22, s38\n"
      "buffer_store_dwordx4 v[40:43], v16, s[12:15], 0 offen sc1\n"
      ".Lk_exit:\n"
      "s_endpgm\n"
      ".Lk_burst6:\n"
      "buffer_load_dwordx4 v[200:203], v10, s[68:71], s43 offen offset:2048 nt\n"
      ".Lk_burst7:\n"
      "buffer_load_dwordx4 v[204:207], v10, s[68:71], s43 offen offset:2176 nt\n"
      ".Lk_burst8:\n"
      "buffer_load_dwordx4 v[208:211], v11, s[72:75], 0 offen nt\n"
      ".Lk_burst9:\n"
      "buffer_load_dwordx4 v[212:215], v11, s[72:75], 0 offen offset:128 nt\n"
      ".Lk_burst10:\n"
      "buffer_load_dwordx4 v[216:219], v11, s[72:75], 0 offen offset:2048 nt\n"
      ".Lk_burst11:\n"
      "buffer_load_dwordx4 v[220:223], v11, s[72:75], 0 offen offset:2176 nt\n"
      ".Lk_burst12:\n"
      "buffer_load_dwordx4 v[224:227], v11, s[72:75], s43 offen nt\n"
      ".Lk_burst13:\n"
      "buffer_load_dwordx4 v[228:231], v11, s[72:75], s43 offen offset:128 nt\n"
      ".Lk_burst14:\n"
      "buffer_load_dwordx4 v[232:235], v11, s[72:75], s43 offen offset:2048 nt\n"
      ".Lk_burst15:\n"
      "buffer_load_dwordx4 v[236:239], v11, s[72:75], s43 offen offset:2176 nt\n"
      "s_branch .Lk_last\n"
      ".Lk_tick:\n"
      "s_add_u32 s66, s66, 280\n"
      ".Lk_tick1:\n"
      "s_memrealtime s[64:65]\n"
      "s_sub_u32 s39, s39, 1\n"
      "s_cbranch_scc1 .Lk_exit\n"
      "s_waitcnt lgkmcnt(0)\n"
      "s_lshl_b32 s64, s64, 4\n"
      "s_sub_u32 s64, s64, s66\n"
      "s_cmp_lt_i32 s64, 0\n"
      "s_cbranch_scc1 .Lk_tick1\n"
      "s_setpc_b64 s[62:63]\n"
      ".Lk_spare:\n"
      "buffer_load_dwordx4 v[136:139], v8, s[24:27], 0 offen offset:2048 nt\n"
      "buffer_load_dwordx4 v[140:143], v8, s[24:27], 0 offen offset:3072 nt\n"
      "s_waitcnt vmcnt(7)\n"
      "ds_write_b128 v12, v[112:115] offset:7760\n"
      "buffer_load_dwordx4 v[176:179], v10, s[68:71], 0 offen nt\n"
      "s_waitcnt vmcnt(7)\n"
      "ds_write_b128 v12, v[116:119] offset:8848\n"
      "buffer_load_dwordx4 v[180:183], v10, s[68:71], 0 offen offset:128 nt\n"
      "s_waitcnt vmcnt(7)\n"
      "ds_write_b128 v12, v[120:123] offset:9936\n"
      "buffer_load_dwordx4 v[184:187], v10, s[68:71], 0 offen offset:2048 nt\n"
      "s_waitcnt vmcnt(7)\n"
      "ds_write_b128 v12, v[124:127] offset:11024\n"
      "buffer_load_dwordx4 v[188:191], v10, s[68:71], 0 offen offset:2176 nt\n"
      "s_waitcnt vmcnt(7)\n"
      "ds_write_b128 v12, v[128:131] offset:25168\n"
      "s_waitcnt vmcnt(6)\n"
      "ds_write_b128 v12, v[132:135] offset:26256\n"
      "s_waitcnt vmcnt(5)\n"
      "ds_write_b128 v12, v[136:139] offset:27344\n"
      "s_waitcnt vmcnt(4)\n"
      "ds_write_b128 v12, v[140:143] offset:28432\n"
      "ds_write_b8 v19, v18 offset:8\n"
      "ds_write_b8 v19, v18 offset:4\n"
      ".Lk_w_7:\n"
      "ds_read_b32 v21, v20 offset:12\n"
      "s_sub_u32 s39, s39, 1\n"
      "s_cbranch_scc1 .Lk_exit\n"
      "s_waitcnt lgkmcnt(0)\n"
      "v_cmp_ne_u32 vcc, s51, v21\n"
      "s_cbranch_vccnz .Lk_w_7\n"
      "buffer_load_dwordx4 v[192:195], v10, s[68:71], s43 offen nt\n"
      "buffer_load_dwordx4 v[196:199], v10, s[68:71], s43 offen offset:128 nt\n"
      "s_add_u32 s66, s66, 2240\n"
      "s_branch .Lk_pace\n"
      ".p2align 12\n"
      ".Lk_comm:\n"
      "s_movk_i32 s43, 0x1000\n"
      "s_movk_i32 s44, 0x3000\n"
      "s_lshr_b32 s45, s2, 3\n"
      "s_and_b32 s47, s2, 1\n"
      "s_bfe_u32 s46, s2, 0x20001\n"
      "s_bfe_u32 s50, s3, 0x20006\n"
      "s_mov_b32 s39, 0x800000\n"
      "s_mov_b32 s51, 0x01010101\n"
      "s_mov_b32 s56, 0x00ff00ff\n"
      "s_mov_b32 s57, 0x00ff00ff\n"
      "s_lshl_b32 s52, s46, 5\n"
      "s_add_u32 s59, s52, s45\n"
      "v_mov_b32 v48, 1\n"
      "v_mov_b32 v49, s50\n"
      "v_mov_b32 v50, 0\n"
      "s_cmp_eq_u32 s50, 0\n"
      "s_cbranch_scc0 .Lk_noln\n"
      "v_and_b32 v1, 60, v5\n"
      "ds_write_b32 v1, v50\n"
      "v_mov_b32 v46, 0\n"
      "v_mov_b32 v47, 0x3f803f80\n"
      "s_waitcnt vmcnt(2)\n"
      "v_dot2c_f32_bf16 v46, v8, v47\n"
      "v_dot2c_f32_bf16 v46, v9, v47\n"
      "v_dot2c_f32_bf16 v46, v10, v47\n"
      "v_dot2c_f32_bf16 v46, v11, v47\n"
      "s_waitcnt vmcnt(1)\n"
      "v_dot2c_f32_bf16 v46, v12, v47\n"
      "v_dot2c_f32_bf16 v46, v13, v47\n"
      "v_dot2c_f32_bf16 v46, v14, v47\n"
      "v_dot2c_f32_bf16 v46, v15, v47\n"
      "v_lshlrev_b32 v16, 16, v8\n"
      "v_and_b32 v17, 0xffff0000, v8\n"
      "v_lshlrev_b32 v18, 16, v9\n"
      "v_add_f32_dpp v46, v46, v46 quad_perm:[1,0,3,2] row_mask:0xf bank_mask:0xf bound_ctrl:1\n"
      "v_and_b32 v19, 0xffff0000, v9\n"
      "v_lshlrev_b32 v20, 16, v10\n"
      "v_add_f32_dpp v46, v46, v46 quad_perm:[2,3,0,1] row_mask:0xf bank_mask:0xf bound_ctrl:1\n"
      "v_and_b32 v21, 0xffff0000, v10\n"
      "v_lshlrev_b32 v22, 16, v11\n"
      "v_add_f32_dpp v46, v46, v46 row_ror:4 row_mask:0xf bank_mask:0xf bound_ctrl:1\n"
      "v_and_b32 v23, 0xffff0000, v11\n"
      "v_lshlrev_b32 v24, 16, v12\n"
      "v_add_f32_dpp v46, v46, v46 row_ror:8 row_mask:0xf bank_mask:0xf bound_ctrl:1\n"
      "v_and_b32 v25, 0xffff0000, v12\n"
      "v_lshlrev_b32 v26, 16, v13\n"
      "v_add_f32_dpp v46, v46, v46 row_bcast:15 row_mask:0xa bank_mask:0xf\n"
      "v_and_b32 v27, 0xffff0000, v13\n"
      "v_lshlrev_b32 v28, 16, v14\n"
      "v_add_f32_dpp v46, v46, v46 row_bcast:31 row_mask:0xc bank_mask:0xf\n"
      "v_and_b32 v29, 0xffff0000, v14\n"
      "v_readlane_b32 s52, v46, 63\n"
      "v_lshlrev_b32 v30, 16, v15\n"
      "v_and_b32 v31, 0xffff0000, v15\n"
      "v_mov_b32 v44, 0xba800000\n"
      "v_mul_f32 v44, s52, v44\n"
      "v_mov_b32 v45, v44\n"
      "v_cvt_pk_bf16_f32 v42, v44, v44\n"
      "v_pk_add_f32 v[32:33], v[16:17], v[44:45]\n"
      "v_pk_mul_f32 v[46:47], v[32:33], v[32:33]\n"
      "v_pk_add_f32 v[32:33], v[18:19], v[44:45]\n"
      "v_pk_fma_f32 v[46:47], v[32:33], v[32:33], v[46:47]\n"
      "v_pk_add_f32 v[32:33], v[20:21], v[44:45]\n"
      "v_pk_fma_f32 v[46:47], v[32:33], v[32:33], v[46:47]\n"
      "v_pk_add_f32 v[32:33], v[22:23], v[44:45]\n"
      "v_pk_fma_f32 v[46:47], v[32:33], v[32:33], v[46:47]\n"
      "v_pk_add_f32 v[32:33], v[24:25], v[44:45]\n"
      "v_pk_fma_f32 v[46:47], v[32:33], v[32:33], v[46:47]\n"
      "v_pk_add_f32 v[32:33], v[26:27], v[44:45]\n"
      "v_pk_fma_f32 v[46:47], v[32:33], v[32:33], v[46:47]\n"
      "v_pk_add_f32 v[32:33], v[28:29], v[44:45]\n"
      "v_pk_fma_f32 v[46:47], v[32:33], v[32:33], v[46:47]\n"
      "v_pk_add_f32 v[32:33], v[30:31], v[44:45]\n"
      "v_pk_fma_f32 v[46:47], v[32:33], v[32:33], v[46:47]\n"
      "v_and_b32 v43, 0xffff0000, v42\n"
      "v_lshlrev_b32 v42, 16, v42\n"
      "v_add_f32 v46, v46, v47\n"
      "v_pk_add_f32 v[16:17], v[16:17], v[42:43]\n"
      "v_cvt_pk_bf16_f32 v8, v16, v17\n"
      "v_add_f32_dpp v46, v46, v46 quad_perm:[1,0,3,2] row_mask:0xf bank_mask:0xf bound_ctrl:1\n"
      "v_pk_add_f32 v[18:19], v[18:19], v[42:43]\n"
      "v_cvt_pk_bf16_f32 v9, v18, v19\n"
      "v_add_f32_dpp v46, v46, v46 quad_perm:[2,3,0,1] row_mask:0xf bank_mask:0xf bound_ctrl:1\n"
      "v_pk_add_f32 v[20:21], v[20:21], v[42:43]\n"
      "v_cvt_pk_bf16_f32 v10, v20, v21\n"
      "v_add_f32_dpp v46, v46, v46 row_ror:4 row_mask:0xf bank_mask:0xf bound_ctrl:1\n"
      "v_pk_add_f32 v[22:23], v[22:23], v[42:43]\n"
      "v_cvt_pk_bf16_f32 v11, v22, v23\n"
      "v_add_f32_dpp v46, v46, v46 row_ror:8 row_mask:0xf bank_mask:0xf bound_ctrl:1\n"
      "v_pk_add_f32 v[24:25], v[24:25], v[42:43]\n"
      "v_cvt_pk_bf16_f32 v12, v24, v25\n"
      "v_add_f32_dpp v46, v46, v46 row_bcast:15 row_mask:0xa bank_mask:0xf\n"
      "v_pk_add_f32 v[26:27], v[26:27], v[42:43]\n"
      "v_cvt_pk_bf16_f32 v13, v26, v27\n"
      "v_add_f32_dpp v46, v46, v46 row_bcast:31 row_mask:0xc bank_mask:0xf\n"
      "v_pk_add_f32 v[28:29], v[28:29], v[42:43]\n"
      "v_readlane_b32 s52, v46, 63\n"
      "v_cvt_pk_bf16_f32 v14, v28, v29\n"
      "v_pk_add_f32 v[30:31], v[30:31], v[42:43]\n"
      "v_cvt_pk_bf16_f32 v15, v30, v31\n"
      "v_mov_b32 v44, 0x3a800000\n"
      "v_mul_f32 v44, s52, v44\n"
      "v_cvt_pk_bf16_f32 v44, v44, v44\n"
      "v_lshlrev_b32 v44, 16, v44\n"
      "v_add_f32 v44, 0x3727c5ac, v44\n"
      "v_cvt_pk_bf16_f32 v44, v44, v44\n"
      "v_lshlrev_b32 v44, 16, v44\n"
      "v_sqrt_f32 v44, v44\n"
      "v_lshlrev_b32 v16, 16, v8\n"
      "v_cvt_pk_bf16_f32 v44, v44, v44\n"
      "v_lshlrev_b32 v44, 16, v44\n"
      "v_rcp_f32 v44, v44\n"
      "v_and_b32 v17, 0xffff0000, v8\n"
      "v_lshlrev_b32 v18, 16, v9\n"
      "v_and_b32 v19, 0xffff0000, v9\n"
      "v_lshlrev_b32 v20, 16, v10\n"
      "v_and_b32 v21, 0xffff0000, v10\n"
      "v_lshlrev_b32 v22, 16, v11\n"
      "v_and_b32 v23, 0xffff0000, v11\n"
      "v_lshlrev_b32 v24, 16, v12\n"
      "v_and_b32 v25, 0xffff0000, v12\n"
      "v_lshlrev_b32 v26, 16, v13\n"
      "v_and_b32 v27, 0xffff0000, v13\n"
      "v_lshlrev_b32 v28, 16, v14\n"
      "v_and_b32 v29, 0xffff0000, v14\n"
      "v_lshlrev_b32 v30, 16, v15\n"
      "v_and_b32 v31, 0xffff0000, v15\n"
      "v_mov_b32 v45, v44\n"
      "v_pk_mul_f32 v[16:17], v[16:17], v[44:45]\n"
      "v_cvt_pk_bf16_f32 v8, v16, v17\n"
      "v_pk_mul_f32 v[18:19], v[18:19], v[44:45]\n"
      "v_cvt_pk_bf16_f32 v9, v18, v19\n"
      "v_pk_mul_f32 v[20:21], v[20:21], v[44:45]\n"
      "v_cvt_pk_bf16_f32 v10, v20, v21\n"
      "v_pk_mul_f32 v[22:23], v[22:23], v[44:45]\n"
      "v_cvt_pk_bf16_f32 v11, v22, v23\n"
      "ds_write_b128 v4, v[8:11] offset:80\n"
      "v_pk_mul_f32 v[24:25], v[24:25], v[44:45]\n"
      "v_cvt_pk_bf16_f32 v12, v24, v25\n"
      "v_pk_mul_f32 v[26:27], v[26:27], v[44:45]\n"
      "v_cvt_pk_bf16_f32 v13, v26, v27\n"
      "v_pk_mul_f32 v[28:29], v[28:29], v[44:45]\n"
      "v_cvt_pk_bf16_f32 v14, v28, v29\n"
      "v_pk_mul_f32 v[30:31], v[30:31], v[44:45]\n"
      "v_cvt_pk_bf16_f32 v15, v30, v31\n"
      "ds_write_b128 v4, v[12:15] offset:96\n"
      "s_waitcnt vmcnt(0)\n"
      "ds_write_b32 v50, v40 offset:64\n"
      ".Lk_noln:\n"
      "s_waitcnt lgkmcnt(0)\n"
      "s_barrier\n"
      "ds_read_b32 v54, v50 offset:64\n"
      "v_lshrrev_b32 v2, 4, v5\n"
      "v_and_b32 v3, 15, v5\n"
      "v_lshlrev_b32 v1, 4, v5\n"
      "v_and_b32 v7, 3, v5\n"
      "s_xor_b32 s52, s47, 1\n"
      "s_lshl2_add_u32 s52, s52, s46\n"
      "s_and_b32 s53, s45, 7\n"
      "s_lshl3_add_u32 s52, s52, s53\n"
      "s_lshl2_add_u32 s53, s52, s50\n"
      "s_lshl_b32 s53, s53, 11\n"
      "s_add_u32 s53, s53, 1347584\n"
      "v_add_u32 v100, s53, v1\n"
      "s_lshr_b32 s53, s52, 2\n"
      "s_lshl_b32 s53, s53, 7\n"
      "s_and_b32 s54, s52, 1\n"
      "s_lshl_b32 s54, s54, 6\n"
      "s_add_u32 s53, s53, s54\n"
      "s_and_b32 s54, s52, 2\n"
      "s_lshl2_add_u32 s53, s54, s53\n"
      "v_lshl_add_u32 v61, v7, 4, s53\n"
      "v_lshl_add_u32 v61, v2, 1, v61\n"
      "v_lshlrev_b32 v101, 6, v2\n"
      "v_lshl_add_u32 v101, v7, 4, v101\n"
      "s_lshr_b32 s52, s45, 3\n"
      "s_cmp_eq_u32 s52, 3\n"
      "s_cselect_b32 s58, 1, 0\n"
      "s_cbranch_scc0 .Lk_nox\n"
      "buffer_load_ushort v60, v61, s[4:7], 0 offen\n"
      ".Lk_nox:\n"
      "s_waitcnt lgkmcnt(0)\n"
      "v_readfirstlane_b32 s34, v54\n"
      "s_cmp_eq_u32 s50, 2\n"
      "s_cbranch_scc1 .Lk_j2\n"
      "s_xor_b32 s52, s59, 64\n"
      "s_lshl1_add_u32 s52, s52, s50\n"
      "s_lshl_b32 s52, s52, 12\n"
      "s_add_u32 s52, s52, 299008\n"
      "v_add_u32 v62, s52, v1\n"
      "s_lshl_b32 s52, s50, 4\n"
      "v_lshl_add_u32 v63, v7, 5, s52\n"
      "s_branch .Lk_jE\n"
      ".Lk_j2:\n"
      "s_mul_i32 s52, s59, 7\n"
      "v_min_u32 v51, 6, v6\n"
      "v_add_u32 v51, s52, v51\n"
      "v_lshrrev_b32 v62, 4, v51\n"
      "v_lshlrev_b32 v62, 12, v62\n"
      "v_and_b32 v52, 56, v5\n"
      "v_lshl_add_u32 v62, v52, 5, v62\n"
      "v_and_b32 v52, 1, v51\n"
      "v_lshl_add_u32 v62, v52, 7, v62\n"
      "v_and_b32 v52, 14, v51\n"
      "v_lshl_add_u32 v62, v52, 3, v62\n"
      "v_add_u32 v62, 69632, v62\n"
      "v_lshlrev_b32 v63, 4, v6\n"
      ".Lk_jE:\n"
      "v_lshl_add_u32 v63, v2, 1, v63\n"
      "s_add_u32 s34, s34, s51\n"
      "s_and_b32 s35, s34, 0xff\n"
      "s_bfe_u32 s36, s34, 0x80008\n"
      "s_bfe_u32 s37, s34, 0x80010\n"
      "s_lshr_b32 s38, s34, 24\n"
      "s_and_b32 s60, s34, 0xffff\n"
      "s_lshr_b32 s61, s34, 16\n"
      "s_and_b32 s20, s35, 63\n"
      "s_and_b32 s21, s36, 63\n"
      "s_and_b32 s22, s37, 63\n"
      "s_and_b32 s23, s38, 63\n"
      "s_lshl4_add_u32 s52, s46, s50\n"
      "s_lshl_b32 s52, s52, 10\n"
      "s_add_u32 s52, s52, 4096\n"
      "v_add_u32 v53, s52, v1\n"
      "s_mul_i32 s52, s50, 4352\n"
      "v_mul_u32_u24 v51, 272, v3\n"
      "v_lshl_add_u32 v51, v2, 4, v51\n"
      "v_add_u32 v54, s52, v51\n"
      "v_mul_u32_u24 v51, 288, v6\n"
      "v_lshl_add_u32 v55, v2, 4, v51\n"
      "v_mul_u32_u24 v51, 144, v6\n"
      "s_lshl_b32 s52, s50, 5\n"
      "v_lshl_add_u32 v56, v2, 3, v51\n"
      "v_add_u32 v56, s52, v56\n"
      "v_and_b32 v52, 8, v5\n"
      "v_lshl_add_u32 v57, v2, 4, v51\n"
      "v_lshl_add_u32 v57, v52, 3, v57\n"
      "v_lshrrev_b32 v51, 2, v3\n"
      "v_lshl_add_u32 v51, v2, 3, v51\n"
      "v_mul_u32_u24 v51, 272, v51\n"
      "v_lshl_add_u32 v51, v7, 3, v51\n"
      "s_lshl_b32 s52, s50, 6\n"
      "v_add_u32 v58, s52, v51\n"
      "s_lshl3_add_u32 s52, s46, s50\n"
      "s_lshl_b32 s52, s52, 15\n"
      "s_lshl_b32 s53, s47, 5\n"
      "s_add_u32 s53, s53, s45\n"
      "s_lshl_b32 s53, s53, 6\n"
      "s_add_u32 s52, s52, s53\n"
      "s_add_u32 s52, s52, 299008\n"
      "v_lshl_add_u32 v59, v52, 11, s52\n"
      "v_and_b32 v51, 4, v5\n"
      "v_lshl_add_u32 v59, v51, 15, v59\n"
      "v_lshl_add_u32 v59, v2, 12, v59\n"
      "v_lshl_add_u32 v59, v7, 4, v59\n"
      "s_mul_i32 s52, s50, 576\n"
      "v_lshrrev_b32 v51, 5, v5\n"
      "v_lshl_add_u32 v51, v51, 5, s52\n"
      "v_lshl_add_u32 v4, v5, 3, v51\n"
      "s_mov_b32 s62, 0x3db504f3\n"
      "s_mov_b32 s63, s62\n"
      "s_mov_b32 s64, 0x3fb8aa3b\n"
      "s_mov_b32 s65, s64\n"
      "v_mov_b32 v96, 0x3f803f80\n"
      "v_mov_b32 v97, 0x3f803f80\n"
      "v_mov_b32 v98, 0x3f803f80\n"
      "v_mov_b32 v99, 0x3f803f80\n"
      ".Lk_w1:\n"
      "ds_read_b32 v51, v50 offset:4\n"
      "s_memrealtime s[30:31]\n"
      "s_sub_u32 s39, s39, 1\n"
      "s_cbranch_scc1 .Lk_exit\n"
      "s_waitcnt lgkmcnt(0)\n"
      "v_cmp_ne_u32 vcc, s51, v51\n"
      "s_sub_u32 s30, s30, s66\n"
      "s_cmpk_lt_u32 s30, 310\n"
      "s_cbranch_vccz .Lk_d1\n"
      "s_cbranch_scc1 .Lk_w1\n"
      ".Lk_d1:\n"
      "buffer_load_dwordx4 v[8:11], v53, s[12:15], 0 offen sc1\n"
      "buffer_load_dwordx4 v[12:15], v53, s[12:15], s43 offen sc1\n"
      "buffer_load_dwordx4 v[16:19], v53, s[12:15], s40 offen sc1\n"
      "buffer_load_dwordx4 v[20:23], v53, s[12:15], s44 offen sc1\n"
      "s_min_u32 s67, s30, 310\n"
      ".Lk_w_9:\n"
      "ds_read_b32 v51, v50 offset:8\n"
      "s_sub_u32 s39, s39, 1\n"
      "s_cbranch_scc1 .Lk_exit\n"
      "s_waitcnt lgkmcnt(0)\n"
      "v_cmp_ne_u32 vcc, s51, v51\n"
      "s_cbranch_vccnz .Lk_w_9\n"
      "ds_read_b128 v[64:67], v54 offset:7760\n"
      "ds_read_b128 v[68:71], v54 offset:7824\n"
      "ds_read_b128 v[72:75], v54 offset:7888\n"
      "ds_read_b128 v[76:79], v54 offset:7952\n"
      "ds_read_b64_tr_b16 v[80:81], v58 offset:25168\n"
      "ds_read_b64_tr_b16 v[82:83], v58 offset:26256\n"
      "ds_read_b64_tr_b16 v[84:85], v58 offset:33872\n"
      "ds_read_b64_tr_b16 v[86:87], v58 offset:34960\n"
      "ds_read_b64_tr_b16 v[88:89], v58 offset:25200\n"
      "ds_read_b64_tr_b16 v[90:91], v58 offset:26288\n"
      "ds_read_b64_tr_b16 v[92:93], v58 offset:33904\n"
      "ds_read_b64_tr_b16 v[94:95], v58 offset:34992\n"
      ".Lk_qchk:\n"
      "s_waitcnt vmcnt(3)\n"
      "v_xor_b32 v8, s20, v8\n"
      "v_xor_b32 v9, s21, v9\n"
      "v_xor_b32 v10, s22, v10\n"
      "v_xor_b32 v11, s23, v11\n"
      "v_or3_b32 v51, v8, v9, v10\n"
      "s_waitcnt vmcnt(2)\n"
      "v_xor_b32 v12, s20, v12\n"
      "v_xor_b32 v13, s21, v13\n"
      "v_xor_b32 v14, s22, v14\n"
      "v_xor_b32 v15, s23, v15\n"
      "v_or3_b32 v51, v11, v12, v51\n"
      "v_or3_b32 v51, v13, v14, v51\n"
      "s_waitcnt vmcnt(1)\n"
      "v_xor_b32 v16, s20, v16\n"
      "v_xor_b32 v17, s21, v17\n"
      "v_xor_b32 v18, s22, v18\n"
      "v_xor_b32 v19, s23, v19\n"
      "v_or3_b32 v51, v15, v16, v51\n"
      "v_or3_b32 v51, v17, v18, v51\n"
      "s_waitcnt vmcnt(0)\n"
      "v_xor_b32 v20, s20, v20\n"
      "v_xor_b32 v21, s21, v21\n"
      "v_xor_b32 v22, s22, v22\n"
      "v_xor_b32 v23, s23, v23\n"
      "v_or3_b32 v51, v19, v20, v51\n"
      "v_or3_b32 v51, v21, v22, v51\n"
      "v_bitop3_b32 v51, v23, v51, 63 bitop3:0xa8\n"
      "v_cmp_ne_u32 vcc, 0, v51\n"
      "s_cbranch_vccnz .Lk_qretry\n"
      "v_pk_add_f32 v[8:9], v[8:9], v[12:13]\n"
      "v_pk_add_f32 v[16:17], v[16:17], v[20:21]\n"
      "v_pk_add_f32 v[10:11], v[10:11], v[14:15]\n"
      "v_pk_add_f32 v[18:19], v[18:19], v[22:23]\n"
      "v_pk_add_f32 v[8:9], v[8:9], v[16:17]\n"
      "v_pk_add_f32 v[10:11], v[10:11], v[18:19]\n"
      "v_cvt_pk_bf16_f32 v8, v8, v9\n"
      "v_cvt_pk_bf16_f32 v9, v10, v11\n"
      "ds_write_b64 v4, v[8:9] offset:4176\n"
      "ds_write_b8 v49, v48 offset:12\n"
      ".Lk_w_11:\n"
      "ds_read_b32 v51, v50 offset:12\n"
      "ds_read_b128 v[32:35], v55 offset:4176\n"
      "ds_read_b128 v[36:39], v55 offset:4240\n"
      "ds_read_b128 v[40:43], v55 offset:4304\n"
      "ds_read_b128 v[44:47], v55 offset:4368\n"
      "s_sub_u32 s39, s39, 1\n"
      "s_cbranch_scc1 .Lk_exit\n"
      "s_waitcnt lgkmcnt(0)\n"
      "v_cmp_ne_u32 vcc, s51, v51\n"
      "s_cbranch_vccnz .Lk_w_11\n"
      "v_mfma_f32_16x16x32_bf16 v[24:27], v[64:67], v[32:35], 0\n"
      "v_mfma_f32_16x16x32_bf16 v[24:27], v[68:71], v[36:39], v[24:27]\n"
      "v_mfma_f32_16x16x32_bf16 v[24:27], v[72:75], v[40:43], v[24:27]\n"
      "v_mfma_f32_16x16x32_bf16 v[24:27], v[76:79], v[44:47], v[24:27]\n"
      "s_nop 7\n"
      "v_cvt_pk_bf16_f32 v8, v24, v25\n"
      "v_cvt_pk_bf16_f32 v9, v26, v27\n"
      "v_lshlrev_b32 v12, 16, v8\n"
      "v_and_b32 v13, 0xffff0000, v8\n"
      "v_lshlrev_b32 v14, 16, v9\n"
      "v_and_b32 v15, 0xffff0000, v9\n"
      "v_pk_mul_f32 v[12:13], v[12:13], s[62:63]\n"
      "v_pk_mul_f32 v[14:15], v[14:15], s[62:63]\n"
      "v_cvt_pk_bf16_f32 v8, v12, v13\n"
      "v_cvt_pk_bf16_f32 v9, v14, v15\n"
      "ds_write_b64 v56, v[8:9] offset:6480\n"
      "ds_write_b8 v49, v48 offset:16\n"
      ".Lk_w_13:\n"
      "ds_read_b32 v51, v50 offset:16\n"
      "ds_read_b128 v[8:11], v57 offset:6480\n"
      "s_sub_u32 s39, s39, 1\n"
      "s_cbranch_scc1 .Lk_exit\n"
      "s_waitcnt lgkmcnt(0)\n"
      "v_cmp_ne_u32 vcc, s51, v51\n"
      "s_cbranch_vccnz .Lk_w_13\n"
      "v_lshlrev_b32 v16, 16, v8\n"
      "v_and_b32 v17, 0xffff0000, v8\n"
      "v_lshlrev_b32 v18, 16, v9\n"
      "v_and_b32 v19, 0xffff0000, v9\n"
      "v_lshlrev_b32 v20, 16, v10\n"
      "v_and_b32 v21, 0xffff0000, v10\n"
      "v_lshlrev_b32 v22, 16, v11\n"
      "v_and_b32 v23, 0xffff0000, v11\n"
      "v_max3_f32 v12, v16, v17, v18\n"
      "v_max3_f32 v13, v19, v20, v21\n"
      "v_max3_f32 v12, v22, v23, v12\n"
      "v_max_f32 v12, v12, v13\n"
      "s_nop 1\n"
      "v_max_f32_dpp v12, v12, v12 row_ror:8 row_mask:0xf bank_mask:0xf bound_ctrl:1\n"
      "v_mov_b32 v13, v12\n"
      "s_nop 1\n"
      "v_permlane16_swap_b32 v12, v13\n"
      "v_max_f32 v12, v12, v13\n"
      "v_mov_b32 v13, v12\n"
      "s_nop 1\n"
      "v_permlane32_swap_b32 v12, v13\n"
      "v_max_f32 v12, v12, v13\n"
      "v_xor_b32 v14, 0x80000000, v12\n"
      "v_xor_b32 v15, 0x80000000, v12\n"
      "v_pk_add_f32 v[16:17], v[16:17], v[14:15]\n"
      "v_pk_add_f32 v[18:19], v[18:19], v[14:15]\n"
      "v_pk_add_f32 v[20:21], v[20:21], v[14:15]\n"
      "v_pk_add_f32 v[22:23], v[22:23], v[14:15]\n"
      "v_pk_mul_f32 v[16:17], v[16:17], s[64:65]\n"
      "v_pk_mul_f32 v[18:19], v[18:19], s[64:65]\n"
      "v_pk_mul_f32 v[20:21], v[20:21], s[64:65]\n"
      "v_pk_mul_f32 v[22:23], v[22:23], s[64:65]\n"
      "v_exp_f32 v16, v16\n"
      "v_exp_f32 v17, v17\n"
      "v_exp_f32 v18, v18\n"
      "v_exp_f32 v19, v19\n"
      "v_exp_f32 v20, v20\n"
      "v_exp_f32 v21, v21\n"
      "v_exp_f32 v22, v22\n"
      "v_exp_f32 v23, v23\n"
      "v_or_b32 v13, s60, v12\n"
      "v_cvt_pk_bf16_f32 v8, v16, v17\n"
      "v_cvt_pk_bf16_f32 v9, v18, v19\n"
      "v_cvt_pk_bf16_f32 v10, v20, v21\n"
      "v_cvt_pk_bf16_f32 v11, v22, v23\n"
      "v_mov_b32_dpp v24, v8 row_ror:8 row_mask:0xf bank_mask:0xf bound_ctrl:1\n"
      "v_mov_b32_dpp v25, v9 row_ror:8 row_mask:0xf bank_mask:0xf bound_ctrl:1\n"
      "v_mov_b32_dpp v26, v10 row_ror:8 row_mask:0xf bank_mask:0xf bound_ctrl:1\n"
      "v_mov_b32_dpp v27, v11 row_ror:8 row_mask:0xf bank_mask:0xf bound_ctrl:1\n"
      "v_mfma_f32_16x16x32_bf16 v[32:35], v[80:83], v[8:11], 0\n"
      "v_mfma_f32_16x16x32_bf16 v[36:39], v[92:95], v[8:11], 0\n"
      "v_mfma_f32_16x16x32_bf16 v[40:43], v[96:99], v[8:11], 0\n"
      "v_mfma_f32_16x16x32_bf16 v[32:35], v[84:87], v[24:27], v[32:35]\n"
      "v_mfma_f32_16x16x32_bf16 v[36:39], v[88:91], v[24:27], v[36:39]\n"
      "v_mfma_f32_16x16x32_bf16 v[40:43], v[96:99], v[24:27], v[40:43]\n"
      "s_cmp_eq_u32 s50, 2\n"
      "s_nop 4\n"
      "v_cvt_pk_bf16_f32 v12, v32, v33\n"
      "v_cvt_pk_bf16_f32 v14, v34, v35\n"
      "v_cvt_pk_bf16_f32 v16, v36, v37\n"
      "v_cvt_pk_bf16_f32 v17, v38, v39\n"
      "v_cvt_pk_bf16_f32 v15, v50, v40\n"
      "v_cndmask_b32 v12, v16, v12, s[56:57]\n"
      "v_cndmask_b32 v14, v17, v14, s[56:57]\n"
      "v_or_b32 v15, s61, v15\n"
      "buffer_store_dwordx4 v[12:15], v59, s[12:15], 0 offen sc1\n"
      "s_cbranch_scc1 .Lk_h\n"
      "s_cmp_eq_u32 s50, 3\n"
      "s_cbranch_scc1 .Lk_w7\n"
      "v_lshlrev_b32 v52, 1, v49\n"
      "v_mov_b32 v54, 0x101\n"
      "s_mul_i32 s55, s67, 110\n"
      "s_lshr_b32 s55, s55, 6\n"
      ".Lk_w_15:\n"
      "s_memrealtime s[30:31]\n"
      "s_sub_u32 s39, s39, 1\n"
      "s_cbranch_scc1 .Lk_exit\n"
      "s_waitcnt lgkmcnt(0)\n"
      "s_sub_u32 s30, s30, s66\n"
      "s_cmp_lt_u32 s30, s55\n"
      "s_cbranch_scc1 .Lk_w_15\n"
      "buffer_load_dwordx4 v[16:19], v62, s[12:15], 0 offen sc1\n"
      "buffer_load_dwordx4 v[20:23], v62, s[12:15], 0 offen offset:1024 sc1\n"
      "buffer_load_dwordx4 v[24:27], v62, s[12:15], 0 offen offset:2048 sc1\n"
      "buffer_load_dwordx4 v[28:31], v62, s[12:15], 0 offen offset:3072 sc1\n"
      "ds_write_b8 v49, v48 offset:36\n"
      ".Lk_ochk:\n"
      "s_waitcnt vmcnt(3)\n"
      "v_xor_b32 v17, s60, v17\n"
      "v_xor_b32 v19, s61, v19\n"
      "s_waitcnt vmcnt(2)\n"
      "v_xor_b32 v21, s60, v21\n"
      "v_xor_b32 v23, s61, v23\n"
      "v_or3_b32 v51, v17, v19, v21\n"
      "s_waitcnt vmcnt(1)\n"
      "v_xor_b32 v25, s60, v25\n"
      "v_xor_b32 v27, s61, v27\n"
      "v_or3_b32 v51, v23, v25, v51\n"
      "s_waitcnt vmcnt(0)\n"
      "v_xor_b32 v29, s60, v29\n"
      "v_xor_b32 v31, s61, v31\n"
      "v_or3_b32 v51, v27, v29, v51\n"
      "v_or_b32 v51, v31, v51\n"
      "v_cmp_ne_u32_sdwa vcc, v51, v50 src0_sel:WORD_0 src1_sel:DWORD\n"
      "s_cbranch_vccnz .Lk_oretry\n"
      "v_max_f32 v8, v17, v21\n"
      "v_max3_f32 v8, v25, v29, v8\n"
      "v_lshlrev_b32 v32, 16, v16\n"
      "v_and_b32 v33, 0xffff0000, v16\n"
      "v_max_f32_dpp v8, v8, v8 row_ror:4 row_mask:0xf bank_mask:0xf bound_ctrl:1\n"
      "v_lshlrev_b32 v34, 16, v18\n"
      "v_and_b32 v35, 0xffff0000, v18\n"
      "v_max_f32_dpp v8, v8, v8 row_ror:8 row_mask:0xf bank_mask:0xf bound_ctrl:1\n"
      "v_mov_b32 v9, v8\n"
      "v_lshlrev_b32 v36, 16, v20\n"
      "v_and_b32 v37, 0xffff0000, v20\n"
      "v_permlane16_swap_b32 v8, v9\n"
      "v_max_f32 v8, v8, v9\n"
      "v_mov_b32 v9, v8\n"
      "v_lshlrev_b32 v38, 16, v22\n"
      "v_and_b32 v39, 0xffff0000, v22\n"
      "v_permlane32_swap_b32 v8, v9\n"
      "v_max_f32 v8, v8, v9\n"
      "v_sub_f32 v10, v17, v8\n"
      "v_sub_f32 v11, v21, v8\n"
      "v_sub_f32 v12, v25, v8\n"
      "v_sub_f32 v13, v29, v8\n"
      "v_pk_mul_f32 v[10:11], v[10:11], s[64:65]\n"
      "v_pk_mul_f32 v[12:13], v[12:13], s[64:65]\n"
      "v_exp_f32 v10, v10\n"
      "v_exp_f32 v11, v11\n"
      "v_exp_f32 v12, v12\n"
      "v_exp_f32 v13, v13\n"
      "v_lshlrev_b32 v40, 16, v24\n"
      "v_and_b32 v41, 0xffff0000, v24\n"
      "v_lshlrev_b32 v42, 16, v26\n"
      "v_and_b32 v43, 0xffff0000, v26\n"
      "v_lshlrev_b32 v44, 16, v28\n"
      "v_and_b32 v45, 0xffff0000, v28\n"
      "v_lshlrev_b32 v46, 16, v30\n"
      "v_and_b32 v47, 0xffff0000, v30\n"
      "v_mul_f32 v14, v10, v19\n"
      "v_fmac_f32 v14, v11, v23\n"
      "v_fmac_f32 v14, v12, v27\n"
      "v_fmac_f32 v14, v13, v31\n"
      "v_mul_f32 v32, v10, v32\n"
      "v_mul_f32 v33, v10, v33\n"
      "v_add_f32_dpp v14, v14, v14 row_ror:4 row_mask:0xf bank_mask:0xf bound_ctrl:1\n"
      "v_mul_f32 v34, v10, v34\n"
      "v_mul_f32 v35, v10, v35\n"
      "v_add_f32_dpp v14, v14, v14 row_ror:8 row_mask:0xf bank_mask:0xf bound_ctrl:1\n"
      "v_mov_b32 v15, v14\n"
      "v_fmac_f32 v32, v11, v36\n"
      "v_fmac_f32 v33, v11, v37\n"
      "v_permlane16_swap_b32 v14, v15\n"
      "v_add_f32 v14, v14, v15\n"
      "v_mov_b32 v15, v14\n"
      "v_fmac_f32 v34, v11, v38\n"
      "v_fmac_f32 v35, v11, v39\n"
      "v_fmac_f32 v32, v12, v40\n"
      "v_fmac_f32 v33, v12, v41\n"
      "v_fmac_f32 v34, v12, v42\n"
      "v_fmac_f32 v35, v12, v43\n"
      "v_fmac_f32 v32, v13, v44\n"
      "v_fmac_f32 v33, v13, v45\n"
      "v_fmac_f32 v34, v13, v46\n"
      "v_fmac_f32 v35, v13, v47\n"
      "v_permlane32_swap_b32 v14, v15\n"
      "v_add_f32 v14, v14, v15\n"
      "v_permlane32_swap_b32 v32, v34\n"
      "v_permlane32_swap_b32 v33, v35\n"
      "v_pk_add_f32 v[32:33], v[32:33], v[34:35]\n"
      "v_rcp_f32 v14, v14\n"
      "s_nop 0\n"
      "v_permlane16_swap_b32 v32, v33\n"
      "v_add_f32 v32, v32, v33\n"
      "s_nop 1\n"
      "v_add_f32_dpp v32, v32, v32 row_ror:4 row_mask:0xf bank_mask:0xf bound_ctrl:1\n"
      "s_nop 1\n"
      "v_add_f32_dpp v32, v32, v32 row_ror:8 row_mask:0xf bank_mask:0xf bound_ctrl:1\n"
      "s_mov_b32 exec_lo, 0x000f000f\n"
      "s_mov_b32 exec_hi, 0x000f000f\n"
      "v_mul_f32 v32, v32, v14\n"
      "v_cvt_pk_bf16_f32 v32, v32, v32\n"
      "ds_write_b16 v63, v32 offset:7640\n"
      "ds_write_b16 v52, v54 offset:24\n"
      "s_mov_b64 exec, -1\n"
      "s_branch .Lk_red\n"
      ".Lk_h:\n"
      "s_mul_i32 s55, s67, 109\n"
      "s_lshr_b32 s55, s55, 6\n"
      ".Lk_w_16:\n"
      "s_memrealtime s[30:31]\n"
      "s_sub_u32 s39, s39, 1\n"
      "s_cbranch_scc1 .Lk_exit\n"
      "s_waitcnt lgkmcnt(0)\n"
      "s_sub_u32 s30, s30, s66\n"
      "s_cmp_lt_u32 s30, s55\n"
      "s_cbranch_scc1 .Lk_w_16\n"
      ".Lk_hretry:\n"
      "buffer_load_dwordx4 v[24:27], v62, s[12:15], 0 offen sc1\n"
      "buffer_load_dwordx4 v[28:31], v62, s[12:15], 0 offen offset:2048 sc1\n"
      "ds_write_b8 v49, v48 offset:36\n"
      "ds_write_b8 v49, v48 offset:37\n"
      "s_sub_u32 s39, s39, 16\n"
      "s_cbranch_scc1 .Lk_exit\n"
      ".Lk_hchk:\n"
      "s_waitcnt vmcnt(1)\n"
      "v_xor_b32 v24, s35, v24\n"
      "v_xor_b32 v25, s36, v25\n"
      "v_xor_b32 v26, s37, v26\n"
      "v_xor_b32 v27, s38, v27\n"
      "v_or3_b32 v51, v24, v25, v26\n"
      "s_waitcnt vmcnt(0)\n"
      "v_xor_b32 v28, s35, v28\n"
      "v_xor_b32 v29, s36, v29\n"
      "v_xor_b32 v30, s37, v30\n"
      "v_xor_b32 v31, s38, v31\n"
      "v_or3_b32 v51, v27, v28, v51\n"
      "v_or3_b32 v51, v29, v30, v51\n"
      "v_or_b32 v51, v31, v51\n"
      "v_cmp_ne_u32_sdwa vcc, v51, v50 src0_sel:BYTE_0 src1_sel:DWORD\n"
      "s_cbranch_vccnz .Lk_hretry\n"
      "v_pk_add_f32 v[24:25], v[24:25], v[28:29]\n"
      "v_pk_add_f32 v[26:27], v[26:27], v[30:31]\n"
      "s_nop 1\n"
      "v_permlane32_swap_b32 v24, v26\n"
      "v_permlane32_swap_b32 v25, v27\n"
      "v_pk_add_f32 v[24:25], v[24:25], v[26:27]\n"
      "s_nop 1\n"
      "v_permlane16_swap_b32 v24, v25\n"
      "v_add_f32 v24, v24, v25\n"
      "s_nop 1\n"
      "v_add_f32_dpp v24, v24, v24 row_ror:8 row_mask:0xf bank_mask:0xf bound_ctrl:1\n"
      "v_cvt_pk_bf16_f32 v24, v50, v24\n"
      "v_mul_f32 v25, 0xbfb8aa3b, v24\n"
      "v_exp_f32 v25, v25\n"
      "v_mov_b32 v51, s51\n"
      "v_add_f32 v25, 1.0, v25\n"
      "v_rcp_f32 v25, v25\n"
      "s_nop 0\n"
      "v_mul_f32 v24, v24, v25\n"
      "v_cvt_pk_bf16_f32 v24, v24, v24\n"
      "ds_write_b16 v63, v24 offset:7632\n"
      "ds_write_b32 v50, v51 offset:20\n"
      ".Lk_red:\n"
      "s_cmp_eq_u32 s58, 0\n"
      "s_cbranch_scc1 .Lk_exit\n"
      "v_lshlrev_b32 v60, 16, v60\n"
      "v_lshl_add_u32 v55, v49, 2, v101\n"
      "s_mov_b32 s60, 0x000f000f\n"
      "s_mov_b32 s61, 0x000f000f\n"
      "s_setprio 0\n"
      ".Lk_w_17:\n"
      "ds_read_b32 v51, v50 offset:24\n"
      "s_sub_u32 s39, s39, 1\n"
      "s_cbranch_scc1 .Lk_exit\n"
      "s_waitcnt lgkmcnt(0)\n"
      "v_cmp_ne_u32 vcc, s51, v51\n"
      "s_cbranch_vccz .Lk_d_18\n"
      "s_sleep 1\n"
      "s_branch .Lk_w_17\n"
      ".Lk_d_18:\n"
      ".Lk_rretry:\n"
      "buffer_load_dwordx4 v[64:67], v100, s[12:15], 0 offen sc1\n"
      "buffer_load_dwordx4 v[68:71], v100, s[12:15], 0 offen offset:1024 sc1\n"
      "s_sub_u32 s39, s39, 16\n"
      "s_cbranch_scc1 .Lk_exit\n"
      ".Lk_rchk:\n"
      "s_waitcnt vmcnt(1)\n"
      "v_xor_b32 v64, s35, v64\n"
      "v_xor_b32 v65, s36, v65\n"
      "v_xor_b32 v66, s37, v66\n"
      "v_xor_b32 v67, s38, v67\n"
      "v_or3_b32 v51, v64, v65, v66\n"
      "s_waitcnt vmcnt(0)\n"
      "v_xor_b32 v68, s35, v68\n"
      "v_xor_b32 v69, s36, v69\n"
      "v_xor_b32 v70, s37, v70\n"
      "v_xor_b32 v71, s38, v71\n"
      "v_or3_b32 v51, v67, v68, v51\n"
      "v_or3_b32 v51, v69, v70, v51\n"
      "v_or_b32 v51, v71, v51\n"
      "v_cmp_ne_u32_sdwa vcc, v51, v50 src0_sel:BYTE_0 src1_sel:DWORD\n"
      "s_cbranch_vccnz .Lk_rretry\n"
      "v_pk_add_f32 v[64:65], v[64:65], v[68:69]\n"
      "v_pk_add_f32 v[66:67], v[66:67], v[70:71]\n"
      "s_nop 1\n"
      "v_permlane32_swap_b32 v64, v66\n"
      "v_permlane32_swap_b32 v65, v67\n"
      "v_pk_add_f32 v[64:65], v[64:65], v[66:67]\n"
      "s_nop 1\n"
      "v_permlane16_swap_b32 v64, v65\n"
      "v_add_f32 v64, v64, v65\n"
      "s_nop 1\n"
      "v_add_f32_dpp v64, v64, v64 row_ror:4 row_mask:0xf bank_mask:0xf bound_ctrl:1\n"
      "s_nop 1\n"
      "v_add_f32_dpp v64, v64, v64 row_ror:8 row_mask:0xf bank_mask:0xf bound_ctrl:1\n"
      "s_mov_b64 exec, s[60:61]\n"
      "ds_write_b32 v55, v64 offset:2128\n"
      "s_mov_b64 exec, 1\n"
      "ds_add_rtn_u32 v51, v50, v48 offset:28\n"
      "s_mov_b64 exec, s[60:61]\n"
      "ds_read_b128 v[68:71], v101 offset:2128\n"
      "s_waitcnt lgkmcnt(0)\n"
      "v_readfirstlane_b32 s52, v51\n"
      "v_pk_add_f32 v[68:69], v[68:69], v[70:71]\n"
      "s_cmp_eq_u32 s52, 3\n"
      "s_cbranch_scc0 .Lk_exit\n"
      "v_add_f32 v64, v68, v69\n"
      "v_add_f32 v64, v64, v60\n"
      "v_cvt_pk_bf16_f32 v64, v64, v64\n"
      "buffer_store_short v64, v61, s[16:19], 0 offen sc1\n"
      "s_endpgm\n"
      ".Lk_w7:\n"
      "s_cmp_lg_u32 s2, 64\n"
      "s_cbranch_scc1 .Lk_red\n"
      "v_lshlrev_b32 v53, 8, v5\n"
      "v_add_u32 v53, 0x5000, v53\n"
      ".Lk_w_19:\n"
      "ds_read_b32 v51, v50 offset:24\n"
      "s_sub_u32 s39, s39, 1\n"
      "s_cbranch_scc1 .Lk_exit\n"
      "s_waitcnt lgkmcnt(0)\n"
      "v_cmp_ne_u32 vcc, s51, v51\n"
      "s_cbranch_vccz .Lk_d_20\n"
      "s_sleep 1\n"
      "s_branch .Lk_w_19\n"
      ".Lk_d_20:\n"
      ".Lk_eretry:\n"
      "buffer_load_dwordx4 v[16:19], v53, s[12:15], 0 offen sc1\n"
      "s_sub_u32 s39, s39, 16\n"
      "s_cbranch_scc1 .Lk_exit\n"
      ".Lk_echk:\n"
      "s_waitcnt vmcnt(0)\n"
      "v_xor_b32 v16, s20, v16\n"
      "v_xor_b32 v17, s21, v17\n"
      "v_xor_b32 v18, s22, v18\n"
      "v_xor_b32 v19, s23, v19\n"
      "v_or3_b32 v51, v16, v17, v18\n"
      "v_bitop3_b32 v51, v19, v51, 63 bitop3:0xa8\n"
      "v_cmp_ne_u32 vcc, 0, v51\n"
      "s_cbranch_vccnz .Lk_eretry\n"
      "s_mov_b64 exec, 1\n"
      "v_mov_b32 v51, s34\n"
      "buffer_store_dword v51, off, s[12:15], 0 sc1\n"
      "s_endpgm\n"
      ".Lk_qretry:\n"
      "v_or3_b32 v51, v8, v9, v10\n"
      "v_bitop3_b32 v51, v11, v51, 63 bitop3:0xa8\n"
      "v_cmp_ne_u32 vcc, 0, v51\n"
      "v_xor_b32 v8, s20, v8\n"
      "v_xor_b32 v9, s21, v9\n"
      "v_xor_b32 v10, s22, v10\n"
      "v_xor_b32 v11, s23, v11\n"
      "s_wqm_b64 exec, vcc\n"
      "s_cbranch_execz .Lk_sk_21\n"
      "buffer_load_dwordx4 v[8:11], v53, s[12:15], 0 offen sc1\n"
      ".Lk_sk_21:\n"
      "s_mov_b64 exec, -1\n"
      "v_or3_b32 v51, v12, v13, v14\n"
      "v_bitop3_b32 v51, v15, v51, 63 bitop3:0xa8\n"
      "v_cmp_ne_u32 vcc, 0, v51\n"
      "v_xor_b32 v12, s20, v12\n"
      "v_xor_b32 v13, s21, v13\n"
      "v_xor_b32 v14, s22, v14\n"
      "v_xor_b32 v15, s23, v15\n"
      "s_wqm_b64 exec, vcc\n"
      "s_cbranch_execz .Lk_sk_22\n"
      "buffer_load_dwordx4 v[12:15], v53, s[12:15], s43 offen sc1\n"
      ".Lk_sk_22:\n"
      "s_mov_b64 exec, -1\n"
      "v_or3_b32 v51, v16, v17, v18\n"
      "v_bitop3_b32 v51, v19, v51, 63 bitop3:0xa8\n"
      "v_cmp_ne_u32 vcc, 0, v51\n"
      "v_xor_b32 v16, s20, v16\n"
      "v_xor_b32 v17, s21, v17\n"
      "v_xor_b32 v18, s22, v18\n"
      "v_xor_b32 v19, s23, v19\n"
      "s_wqm_b64 exec, vcc\n"
      "s_cbranch_execz .Lk_sk_23\n"
      "buffer_load_dwordx4 v[16:19], v53, s[12:15], s40 offen sc1\n"
      ".Lk_sk_23:\n"
      "s_mov_b64 exec, -1\n"
      "v_or3_b32 v51, v20, v21, v22\n"
      "v_bitop3_b32 v51, v23, v51, 63 bitop3:0xa8\n"
      "v_cmp_ne_u32 vcc, 0, v51\n"
      "v_xor_b32 v20, s20, v20\n"
      "v_xor_b32 v21, s21, v21\n"
      "v_xor_b32 v22, s22, v22\n"
      "v_xor_b32 v23, s23, v23\n"
      "s_wqm_b64 exec, vcc\n"
      "s_cbranch_execz .Lk_sk_24\n"
      "buffer_load_dwordx4 v[20:23], v53, s[12:15], s44 offen sc1\n"
      ".Lk_sk_24:\n"
      "s_mov_b64 exec, -1\n"
      "s_sub_u32 s39, s39, 16\n"
      "s_cbranch_scc1 .Lk_exit\n"
      "s_waitcnt vmcnt(0)\n"
      "s_branch .Lk_qchk\n"
      ".Lk_oretry:\n"
      "v_or_b32 v51, v17, v19\n"
      "v_cmp_ne_u32_sdwa vcc, v51, v50 src0_sel:WORD_0 src1_sel:DWORD\n"
      "v_xor_b32 v17, s60, v17\n"
      "v_xor_b32 v19, s61, v19\n"
      "s_wqm_b64 exec, vcc\n"
      "s_cbranch_execz .Lk_sk_25\n"
      "buffer_load_dwordx4 v[16:19], v62, s[12:15], 0 offen sc1\n"
      ".Lk_sk_25:\n"
      "s_mov_b64 exec, -1\n"
      "v_or_b32 v51, v21, v23\n"
      "v_cmp_ne_u32_sdwa vcc, v51, v50 src0_sel:WORD_0 src1_sel:DWORD\n"
      "v_xor_b32 v21, s60, v21\n"
      "v_xor_b32 v23, s61, v23\n"
      "s_wqm_b64 exec, vcc\n"
      "s_cbranch_execz .Lk_sk_26\n"
      "buffer_load_dwordx4 v[20:23], v62, s[12:15], 0 offen offset:1024 sc1\n"
      ".Lk_sk_26:\n"
      "s_mov_b64 exec, -1\n"
      "v_or_b32 v51, v25, v27\n"
      "v_cmp_ne_u32_sdwa vcc, v51, v50 src0_sel:WORD_0 src1_sel:DWORD\n"
      "v_xor_b32 v25, s60, v25\n"
      "v_xor_b32 v27, s61, v27\n"
      "s_wqm_b64 exec, vcc\n"
      "s_cbranch_execz .Lk_sk_27\n"
      "buffer_load_dwordx4 v[24:27], v62, s[12:15], 0 offen offset:2048 sc1\n"
      ".Lk_sk_27:\n"
      "s_mov_b64 exec, -1\n"
      "v_or_b32 v51, v29, v31\n"
      "v_cmp_ne_u32_sdwa vcc, v51, v50 src0_sel:WORD_0 src1_sel:DWORD\n"
      "v_xor_b32 v29, s60, v29\n"
      "v_xor_b32 v31, s61, v31\n"
      "s_wqm_b64 exec, vcc\n"
      "s_cbranch_execz .Lk_sk_28\n"
      "buffer_load_dwordx4 v[28:31], v62, s[12:15], 0 offen offset:3072 sc1\n"
      ".Lk_sk_28:\n"
      "s_mov_b64 exec, -1\n"
      "s_sub_u32 s39, s39, 16\n"
      "s_cbranch_scc1 .Lk_exit\n"
      "s_waitcnt vmcnt(0)\n"
      "s_branch .Lk_ochk\n"
      ".Lk_end:\n"
      :
      : "{s[0:1]}"(__builtin_amdgcn_kernarg_segment_ptr()), "{s2}"(blockIdx.x), "{v0}"(threadIdx.x),
        "s"((unsigned)(size_t)(__attribute__((address_space(3))) char*)lds_)
      : "v1", "v2", "v3", "v4", "v5", "v6", "v7", "v8", "v9", "v10", "v11", "v12", "v13", "v14", "v15", "v16",
        "v17", "v18", "v19", "v20", "v21", "v22", "v23", "v24", "v25", "v26", "v27", "v28", "v29", "v30", "v31", "v32",
        "v33", "v34", "v35", "v36", "v37", "v38", "v39", "v40", "v41", "v42", "v43", "v44", "v45", "v46", "v47", "v48",
        "v49", "v50", "v51", "v52", "v53", "v54", "v55", "v56", "v57", "v58", "v59", "v60", "v61", "v62", "v63", "v64",
        "v65", "v66", "v67", "v68", "v69", "v70", "v71", "v72", "v73", "v74", "v75", "v76", "v77", "v78", "v79", "v80",
        "v81", "v82", "v83", "v84", "v85", "v86", "v87", "v88", "v89", "v90", "v91", "v92", "v93", "v94", "v95", "v96",
        "v97", "v98", "v99", "v100", "v101", "v102", "v103", "v104", "v105", "v106", "v107", "v108", "v109", "v110", "v111", "v112",
        "v113", "v114", "v115", "v116", "v117", "v118", "v119", "v120", "v121", "v122", "v123", "v124", "v125", "v126", "v127", "v128",
        "v129", "v130", "v131", "v132", "v133", "v134", "v135", "v136", "v137", "v138", "v139", "v140", "v141", "v142", "v143", "v144",
        "v145", "v146", "v147", "v148", "v149", "v150", "v151", "v152", "v153", "v154", "v155", "v156", "v157", "v158", "v159", "v160",
        "v161", "v162", "v163", "v164", "v165", "v166", "v167", "v168", "v169", "v170", "v171", "v172", "v173", "v174", "v175", "v176",
        "v177", "v178", "v179", "v180", "v181", "v182", "v183", "v184", "v185", "v186", "v187", "v188", "v189", "v190", "v191", "v192",
        "v193", "v194", "v195", "v196", "v197", "v198", "v199", "v200", "v201", "v202", "v203", "v204", "v205", "v206", "v207", "v208",
        "v209", "v210", "v211", "v212", "v213", "v214", "v215", "v216", "v217", "v218", "v219", "v220", "v221", "v222", "v223", "v224",
        "v225", "v226", "v227", "v228", "v229", "v230", "v231", "v232", "v233", "v234", "v235", "v236", "v237", "v238", "v239", "s3",
        "s4", "s5", "s6", "s7", "s8", "s9", "s10", "s11", "s12", "s13", "s14", "s15", "s16", "s17", "s18", "s19",
        "s20", "s21", "s22", "s23", "s24", "s25", "s26", "s27", "s28", "s29", "s30", "s31", "s34", "s35", "s36", "s37",
        "s38", "s39", "s40", "s41", "s42", "s43", "s44", "s45", "s46", "s47", "s48", "s49", "s50", "s51", "s52", "s53",
        "s54", "s55", "s56", "s57", "s58", "s59", "s60", "s61", "s62", "s63", "s64", "s65", "s66", "s67", "s68", "s69",
        "s70", "s71", "s72", "s73", "s74", "s75", "vcc", "scc", "memory");
}

extern "C" void kernel_launch(void* const* d_in, const int* in_sizes, int n_in, void* d_out, int out_size, void* d_ws, size_t ws_size,
                              hipStream_t stream) {
  const unsigned F = 0x00020000u;
  fused_decode<<<256, 512, 0, stream>>>(d_in[0], 2048u, F, d_in[3], 8388608u, F, d_ws, 1871872u, F, d_out, 2048u, F,
                                        d_in[1], 67108864u, F, d_in[2], 67108864u, F, d_in[6], 7340032u, F, d_in[7], 7340032u, F,
                                        d_in[4], 8388608u, F);
}
```
